```python
import torch

def _softmax(x, axis=-1):
    return torch.softmax(x, dim=axis)
def _gelu(x):
    return torch.nn.functional.gelu(x, approximate="tanh")
def _silu(x):
    return torch.nn.functional.silu(x)
def _sigmoid(x):
    return torch.sigmoid(x)
def _layernorm(x, axis=-1, eps=1e-5):
    m = x.mean(dim=axis, keepdim=True)
    v = x.var(dim=axis, keepdim=True, unbiased=False)
    return (x - m) / torch.sqrt(v + eps)
def _scatter_add(tgt, idx, src, axis=0):
    return tgt.clone().index_add_(axis, idx, src)
def _segment_sum(src, seg, n):
    out = torch.zeros((n,) + src.shape[1:], dtype=src.dtype)
    return out.index_add_(0, seg, src)
def _pad(x, pw):
    flat = tuple(v for pair in reversed(pw) for v in pair)
    return torch.nn.functional.pad(x, flat)

kernel_name = 'recurrent_scan__gqa_block_cached__dense_activation_relu__decode'

def setup_inputs():

    _w = (lambda a: (lambda t: t.to(torch.bfloat16) if t.is_floating_point() else t)(torch.as_tensor(a)))
    def _A_setup():
        B, Sq, D = 2, 1, 512
        Hq, Hkv, Sk, Dh = 32, 8, 32768, 128
        assert Hq % Hkv == 0, (Hq, Hkv)
        return {
            "x": _w((lambda *s: torch.randn(*s, dtype=torch.bfloat16) * 0.05)(B, Sq, D)),
            "k_cache": _w((lambda *s: torch.randn(*s, dtype=torch.bfloat16) * 0.8)(B, Hkv, Sk, Dh)),
            "v_cache": _w((lambda *s: torch.randn(*s, dtype=torch.bfloat16) * 0.8)(B, Hkv, Sk, Dh)),
            "wq": _w((lambda *s: torch.randn(*s, dtype=torch.bfloat16) * 0.8)(D, Hq * Dh) * (D ** -0.5)),
            "wo": _w((lambda *s: torch.randn(*s, dtype=torch.bfloat16) * 0.8)(Hq * Dh, D) * ((Hq * Dh) ** -0.5)),
        }
    _A_in = _A_setup()

    def _B_setup():
        B, S, D, Dff = 2, 1, 512, 1792
        return {
            "x": _w((lambda *s: torch.randn(*s, dtype=torch.bfloat16) * 0.05)(B, S, D)),
            "w1": _w((lambda *s: torch.randn(*s, dtype=torch.bfloat16) * 0.2)(D, Dff)),
            "w2": _w((lambda *s: torch.randn(*s, dtype=torch.bfloat16) * 0.2)(Dff, D)),
        }
    _B_in = _B_setup()

    return {
        "A_x": _A_in["x"],
        "A_k_cache": _A_in["k_cache"],
        "A_v_cache": _A_in["v_cache"],
        "A_wq": _A_in["wq"],
        "A_wo": _A_in["wo"],
        "B_x": _B_in["x"],
        "B_w1": _B_in["w1"],
        "B_w2": _B_in["w2"],
    }

def reference(A_x, A_k_cache, A_v_cache, A_wq, A_wo, B_x, B_w1, B_w2):
    def _A_ref(x, k_cache, v_cache, wq, wo):
        B, Sq, D = x.shape
        Hq, Dh = 32, 128
        Hkv = k_cache.shape[1]
        rep = Hq // Hkv
        q = (lambda x, *d: x.permute(*d))((x @ wq).reshape(B, Sq, Hq, Dh), 0, 2, 1, 3)
        k = (lambda x, n, axis: torch.repeat_interleave(x, n, dim=axis))(k_cache, rep, axis=1)
        v = (lambda x, n, axis: torch.repeat_interleave(x, n, dim=axis))(v_cache, rep, axis=1)
        scores = torch.einsum("bhqd,bhkd->bhqk", q, k) * (Dh ** -0.5)
        p = _softmax(scores, axis=-1)
        o = torch.einsum("bhqk,bhkd->bhqd", p, v)
        return (lambda x, *d: x.permute(*d))(o, 0, 2, 1, 3).reshape(B, Sq, Hq * Dh) @ wo

    def _B_ref(x, w1, w2):
        h = torch.einsum("bsd,df->bsf", x, w1)
        zero = torch.zeros_like(h)
        h = torch.maximum(h, zero)
        return torch.einsum("bsf,fd->bsd", h, w2)

    x = A_x
    xn = _layernorm(x, axis=-1)
    s = _A_ref(xn, A_k_cache, A_v_cache, A_wq, A_wo)
    g = _silu(_B_ref(xn, B_w1, B_w2))
    return x + s * g
```

```cpp
#include <hip/hip_runtime.h>
#include <hip/hip_bf16.h>
#include <stdint.h>

typedef unsigned u32x4 __attribute__((ext_vector_type(4)));
typedef unsigned u32x2 __attribute__((ext_vector_type(2)));
typedef float f32x4 __attribute__((ext_vector_type(4)));
typedef float f32x2 __attribute__((ext_vector_type(2)));
typedef __bf16 bf16x2 __attribute__((ext_vector_type(2)));
typedef __bf16 bf16x8 __attribute__((ext_vector_type(8)));
typedef short s16x4 __attribute__((ext_vector_type(4)));
typedef unsigned long long u64;

namespace {

constexpr int NWG = 256, NTHR = 512, NWAVE = 8;
constexpr int SK = 32768, DH = 128, DM = 512, DFF = 1792, NQ = 4096;
constexpr int TPW = 16;
constexpr int DEPTH = 4;
constexpr int NFF = DFF / 8;
constexpr int AP_STRIDE = 144;
constexpr unsigned WS_EPOCH = 0;
constexpr unsigned WS_QG = 4096;
constexpr unsigned WS_AP = WS_QG + 2 * NQ * 8;
constexpr unsigned WS_YP = WS_AP + NWG * 4 * AP_STRIDE * 8;
constexpr unsigned WS_SP = WS_YP + NFF * 1024 * 8;
constexpr unsigned WS_END = WS_SP + NWG * 1024 * 8;
constexpr unsigned WS_DBG = 8u << 20;
constexpr unsigned SPIN_CAP = 1u << 16;
constexpr int AUX_NT = 2, AUX_SC1 = 16;
constexpr float SOFT_C = 0.08838834764831845f * 1.4426950408889634f;

__device__ __forceinline__ float bf_lo(unsigned u) { return __uint_as_float(u << 16); }
__device__ __forceinline__ float bf_hi(unsigned u) { return __uint_as_float(u & 0xffff0000u); }
__device__ __forceinline__ unsigned cvtpk(float lo, float hi) {
  return __builtin_bit_cast(unsigned, __builtin_convertvector(f32x2{lo, hi}, bf16x2));
}
__device__ __forceinline__ float rbf(float x) { return bf_lo(cvtpk(x, x)); }
template <int CTRL> __device__ __forceinline__ float dpp(float x) {
  return __builtin_bit_cast(float, __builtin_amdgcn_mov_dpp(__builtin_bit_cast(int, x), CTRL, 0xf, 0xf, true));
}
constexpr int ROR1 = 0x121, ROR2 = 0x122, ROR4 = 0x124, ROR8 = 0x128;
__device__ __forceinline__ float xrow_max(float x) {
  auto s = __builtin_amdgcn_permlane16_swap(__float_as_uint(x), __float_as_uint(x), false, false);
  x = fmaxf(__uint_as_float(s[0]), __uint_as_float(s[1]));
  auto t = __builtin_amdgcn_permlane32_swap(__float_as_uint(x), __float_as_uint(x), false, false);
  return fmaxf(__uint_as_float(t[0]), __uint_as_float(t[1]));
}
__device__ __forceinline__ float xrow_sum(float x) {
  auto s = __builtin_amdgcn_permlane16_swap(__float_as_uint(x), __float_as_uint(x), false, false);
  x = __uint_as_float(s[0]) + __uint_as_float(s[1]);
  auto t = __builtin_amdgcn_permlane32_swap(__float_as_uint(x), __float_as_uint(x), false, false);
  return __uint_as_float(t[0]) + __uint_as_float(t[1]);
}
__device__ __forceinline__ float wave_sum(float x) {
  x += dpp<ROR8>(x); x += dpp<ROR4>(x); x += dpp<ROR2>(x); x += dpp<ROR1>(x);
  return xrow_sum(x);
}
__device__ __forceinline__ float tr32(float a, float b) {
  auto t = __builtin_amdgcn_permlane32_swap(__float_as_uint(a), __float_as_uint(b), false, false);
  return __uint_as_float(t[0]) + __uint_as_float(t[1]);
}
__device__ __forceinline__ float tr16(float a, float b) {
  auto t = __builtin_amdgcn_permlane16_swap(__float_as_uint(a), __float_as_uint(b), false, false);
  return __uint_as_float(t[0]) + __uint_as_float(t[1]);
}
__device__ __forceinline__ float tr8(float a, float b, bool bit3) {
  const float keep = bit3 ? b : a, send = bit3 ? a : b;
  return keep + dpp<ROR8>(send);
}
__device__ __forceinline__ unsigned f24(float x) { return (__float_as_uint(x) + 0x80u) >> 8; }
__device__ __forceinline__ u32x2 gr24(unsigned payload24, unsigned tlo8, unsigned thi) { return u32x2{payload24 | tlo8, thi}; }
__device__ __forceinline__ bool ok24(unsigned lo, unsigned hi, unsigned tlo8, unsigned thi) { return ((lo & 0xff000000u) == tlo8) & (hi == thi); }
__device__ __forceinline__ float val24(unsigned lo) { return __uint_as_float(lo << 8); }

struct Params {
  const unsigned short* x; const unsigned short* k; const unsigned short* v; const unsigned short* wq; const unsigned short* wo;
  const unsigned short* w1; const unsigned short* w2; unsigned short* out; unsigned char* ws;
};

#define STAMP(i) do {} while (0)

__global__ void __launch_bounds__(NTHR, 2) fused_decode_kernel(Params p) {
  __shared__ __attribute__((aligned(16))) float lds[8192];
  float* red_q = lds;
  float* red_h = lds + 1024;
  float* h_s = lds + 1536;
  unsigned short* q_s = reinterpret_cast<unsigned short*>(lds + 1600);
  float* mrg_o = lds + 2048;
  float* mrg_m = lds + 6144;
  float* mrg_l = lds + 6176;
  float* c_acc = lds + 6400;
  unsigned* c_st = reinterpret_cast<unsigned*>(lds + 6912);
  float* fin_y = lds + 7100;
  float* fin_s = lds + 7140;

  const int w = blockIdx.x, tid = threadIdx.x, lane = tid & 63;
  const int wave = __builtin_amdgcn_readfirstlane(tid >> 6);
  const int kvh = w & 7, bat = (w >> 3) & 1, split = w >> 4, idx = w >> 3;
  STAMP(0);

  const __amdgpu_buffer_rsrc_t wsr = __builtin_amdgcn_make_buffer_rsrc((void*)p.ws, 0, WS_END, 0x00020000);
  const __amdgpu_buffer_rsrc_t kr = __builtin_amdgcn_make_buffer_rsrc((void*)p.k, 0, 2u * 8 * SK * DH * 2, 0x00020000);
  const __amdgpu_buffer_rsrc_t vr = __builtin_amdgcn_make_buffer_rsrc((void*)p.v, 0, 2u * 8 * SK * DH * 2, 0x00020000);

  const u32x4 xa = *reinterpret_cast<const u32x4*>(p.x + 8 * lane);
  const u32x4 xb = *reinterpret_cast<const u32x4*>(p.x + DM + 8 * lane);
  const int r0 = tid >> 1, cq = tid & 1;
  const unsigned short* wqp = p.wq + (size_t)r0 * NQ + kvh * 512 + idx * 16 + cq * 8;
  const u32x4 wqa = __builtin_nontemporal_load(reinterpret_cast<const u32x4*>(wqp));
  const u32x4 wqb = __builtin_nontemporal_load(reinterpret_cast<const u32x4*>(wqp + (size_t)256 * NQ));
  const unsigned short xs00 = p.x[r0], xs01 = p.x[r0 + 256], xs10 = p.x[DM + r0], xs11 = p.x[DM + r0 + 256];
  const unsigned short xt0 = p.x[tid], xt1 = p.x[DM + tid];
  const u64 epoch = __hip_atomic_load(reinterpret_cast<const u64*>(p.ws + WS_EPOCH), __ATOMIC_RELAXED, __HIP_MEMORY_SCOPE_AGENT);
  const bool has_ff = w < NFF;
  const int wf = has_ff ? w : 0;
  const u32x4 w1v = __builtin_nontemporal_load(reinterpret_cast<const u32x4*>(p.w1 + (size_t)tid * DFF + wf * 8));
  const int frow = lane >> 3, fcc = lane & 7;
  const u32x4 w2v = __builtin_nontemporal_load(reinterpret_cast<const u32x4*>(p.w2 + (size_t)(wf * 8 + frow) * DM + wave * 64 + fcc * 8));
  __builtin_amdgcn_sched_barrier(0);

  const int n16 = lane & 15, g4 = lane >> 4;
  const unsigned kvoff = (unsigned)(((n16 & 3) * 4 + (n16 >> 2)) * 256 + g4 * 16);
  const unsigned vvoff = (unsigned)lane * 16;
  const unsigned sbase = (unsigned)((bat * 8 + kvh) * SK + split * 2048 + wave * 16) * (DH * 2);
  u32x4 kb[DEPTH][4], vb[DEPTH][4];
#define ISSUE(d_, i_) do { const unsigned so_ = sbase + (unsigned)(i_) * (NWAVE * 16 * DH * 2); \
    _Pragma("unroll") for (int s_ = 0; s_ < 4; ++s_) kb[d_][s_] = __builtin_amdgcn_raw_buffer_load_b128(kr, kvoff + s_ * 64, so_, AUX_NT); \
    _Pragma("unroll") for (int s_ = 0; s_ < 4; ++s_) vb[d_][s_] = __builtin_amdgcn_raw_buffer_load_b128(vr, vvoff + s_ * 1024, so_, AUX_NT); } while (0)
  ISSUE(0, 0);
  const unsigned short* wop = p.wo + (size_t)(kvh * 512 + idx * 16 + frow) * DM + wave * 64 + fcc * 8;
  const u32x4 woa = __builtin_nontemporal_load(reinterpret_cast<const u32x4*>(wop));
  const u32x4 wob = __builtin_nontemporal_load(reinterpret_cast<const u32x4*>(wop + 8 * DM));
  __builtin_amdgcn_sched_barrier(0);

  float mean[2], rden[2];
  {
    float e0[8], e1[8];
#pragma unroll
    for (int j = 0; j < 4; ++j) { e0[2 * j] = bf_lo(xa[j]); e0[2 * j + 1] = bf_hi(xa[j]); e1[2 * j] = bf_lo(xb[j]); e1[2 * j + 1] = bf_hi(xb[j]); }
    float s0 = 0.f, s1 = 0.f;
#pragma unroll
    for (int j = 0; j < 8; ++j) { s0 += e0[j]; s1 += e1[j]; }
    s0 = wave_sum(s0) * (1.f / DM); s1 = wave_sum(s1) * (1.f / DM);
    float v0 = 0.f, v1 = 0.f;
#pragma unroll
    for (int j = 0; j < 8; ++j) { const float a = e0[j] - s0, b = e1[j] - s1; v0 = fmaf(a, a, v0); v1 = fmaf(b, b, v1); }
    v0 = wave_sum(v0) * (1.f / DM); v1 = wave_sum(v1) * (1.f / DM);
    mean[0] = rbf(s0); mean[1] = rbf(s1);
    rden[0] = rbf(sqrtf(rbf(rbf(v0) + 1e-5f))); rden[1] = rbf(sqrtf(rbf(rbf(v1) + 1e-5f)));
  }
  auto xnorm = [&](unsigned short xv, int b) { return rbf(rbf(__uint_as_float((unsigned)xv << 16) - mean[b]) / rden[b]); };

  const u64 tag = epoch + 1;
  const unsigned t48_lo = (unsigned)(tag << 16), t48_hi = (unsigned)(tag >> 16);
  const unsigned t40_lo = (unsigned)(tag << 24), t40_hi = (unsigned)(tag >> 8);

  {
    const float a00 = xnorm(xs00, 0), a01 = xnorm(xs01, 0), a10 = xnorm(xs10, 1), a11 = xnorm(xs11, 1);
    float qa[16];
#pragma unroll
    for (int j = 0; j < 4; ++j) {
      const float l0 = bf_lo(wqa[j]), h0 = bf_hi(wqa[j]), l1 = bf_lo(wqb[j]), h1 = bf_hi(wqb[j]);
      qa[2 * j] = fmaf(a01, l1, a00 * l0); qa[2 * j + 1] = fmaf(a01, h1, a00 * h0);
      qa[8 + 2 * j] = fmaf(a11, l1, a10 * l0); qa[8 + 2 * j + 1] = fmaf(a11, h1, a10 * h0);
    }
#pragma unroll
    for (int j = 0; j < 16; ++j) { float t = qa[j]; t += dpp<ROR8>(t); t += dpp<ROR4>(t); t += dpp<ROR2>(t); qa[j] = t; }
    if (n16 < 2) {
      float* d = red_q + ((wave * 4 + g4) * 2 + n16) * 16;
#pragma unroll
      for (int j = 0; j < 16; j += 4) *reinterpret_cast<f32x4*>(d + j) = f32x4{qa[j], qa[j + 1], qa[j + 2], qa[j + 3]};
    }
    const float b0 = xnorm(xt0, 0), b1 = xnorm(xt1, 1);
    float ha[16];
#pragma unroll
    for (int j = 0; j < 4; ++j) {
      const float l0 = bf_lo(w1v[j]), h0 = bf_hi(w1v[j]);
      ha[2 * j] = b0 * l0; ha[2 * j + 1] = b0 * h0; ha[8 + 2 * j] = b1 * l0; ha[8 + 2 * j + 1] = b1 * h0;
    }
#pragma unroll
    for (int j = 0; j < 16; ++j) { float t = ha[j]; t += dpp<ROR8>(t); t += dpp<ROR4>(t); t += dpp<ROR2>(t); t += dpp<ROR1>(t); ha[j] = t; }
    if (n16 == 0) {
      float* d = red_h + (wave * 4 + g4) * 16;
#pragma unroll
      for (int j = 0; j < 16; j += 4) *reinterpret_cast<f32x4*>(d + j) = f32x4{ha[j], ha[j + 1], ha[j + 2], ha[j + 3]};
    }
  }
  __syncthreads();
  if (wave == 0) {
    const int o = lane & 31, hf = lane >> 5;
    float s = 0.f;
#pragma unroll
    for (int r = 0; r < 16; ++r) s += red_q[(hf * 16 + r) * 32 + o];
    auto t = __builtin_amdgcn_permlane32_swap(__float_as_uint(s), __float_as_uint(s), false, false);
    s = __uint_as_float(t[0]) + __uint_as_float(t[1]);
    if (lane < 32) {
      const int c = o >> 4, b = (o >> 3) & 1, e = o & 7;
      const unsigned qb = cvtpk(s, s) & 0xffffu;
      __builtin_amdgcn_raw_buffer_store_b64(u32x2{qb | t48_lo, t48_hi}, wsr, WS_QG + (unsigned)(b * NQ + kvh * 512 + idx * 16 + c * 8 + e) * 8, 0, AUX_SC1);
    }
  } else if (wave == 1) {
    float s = 0.f;
#pragma unroll
    for (int r = 0; r < 8; ++r) s += red_h[(g4 * 8 + r) * 16 + n16];
    s = xrow_sum(s);
    if (lane < 16) h_s[lane] = fmaxf(rbf(s), 0.f);
  }
  STAMP(1);
  __builtin_amdgcn_sched_barrier(0);
  ISSUE(1, 1);
  __builtin_amdgcn_sched_barrier(0);
  const unsigned qoff = WS_QG + (unsigned)(bat * NQ + kvh * 512 + tid) * 8;
  u32x2 qg = __builtin_amdgcn_raw_buffer_load_b64(wsr, qoff, 0, AUX_SC1);
  __builtin_amdgcn_sched_barrier(0);
  ISSUE(2, 2);
  ISSUE(3, 3);
  __builtin_amdgcn_sched_barrier(0);
  __syncthreads();
  if (has_ff) {
    const float h0 = h_s[frow], h1 = h_s[8 + frow];
    float ya[8];
#pragma unroll
    for (int j = 0; j < 4; ++j) {
      const float l0 = bf_lo(w2v[j]), hh = bf_hi(w2v[j]);
      ya[2 * j] = tr32(h0 * l0, h1 * l0); ya[2 * j + 1] = tr32(h0 * hh, h1 * hh);
    }
#pragma unroll
    for (int j = 0; j < 4; ++j) ya[j] = tr16(ya[j], ya[j + 4]);
    const bool b3 = (lane >> 3) & 1;
    const float y0 = tr8(ya[0], ya[2], b3), y1 = tr8(ya[1], ya[3], b3);
    const unsigned col = (unsigned)((lane >> 5) * DM + wave * 64 + fcc * 8 + ((lane >> 4) & 1) * 4 + (b3 ? 2 : 0));
    __builtin_amdgcn_raw_buffer_store_b128(u32x4{f24(y0) | t40_lo, t40_hi, f24(y1) | t40_lo, t40_hi}, wsr, WS_YP + ((unsigned)w * 1024 + col) * 8, 0, AUX_SC1);
  }
  {
    unsigned spins = 0;
    while (!__all(((qg[0] & 0xffff0000u) == t48_lo) & (qg[1] == t48_hi))) {
      if (++spins > SPIN_CAP) break;
      __builtin_amdgcn_s_sleep(1);
      asm volatile("" ::: "memory");
      qg = __builtin_amdgcn_raw_buffer_load_b64(wsr, qoff, 0, AUX_SC1);
    }
    q_s[tid] = (unsigned short)(qg[0] & 0xffffu);
  }
  __syncthreads();
  bf16x8 qf[4];
#pragma unroll
  for (int s = 0; s < 4; ++s) qf[s] = *reinterpret_cast<const bf16x8*>(q_s + (n16 >> 2) * DH + s * 32 + g4 * 8);
  STAMP(2);

  const unsigned msk0 = (n16 & 3) == 0 ? 0x0000ffffu : (n16 & 3) == 1 ? 0xffff0000u : 0u;
  const unsigned msk1 = (n16 & 3) == 2 ? 0x0000ffffu : (n16 & 3) == 3 ? 0xffff0000u : 0u;
  float m_run = -INFINITY, l_run = 0.f;
  f32x4 olo = {0.f, 0.f, 0.f, 0.f}, ohi = {0.f, 0.f, 0.f, 0.f};
  u32x4 yg = {0u, 0u, 0u, 0u};
  const unsigned yoff = WS_YP + ((unsigned)(tid >> 1) * 1024 + (unsigned)w * 4 + (tid & 1) * 2) * 8;
#pragma unroll
  for (int i = 0; i < TPW; ++i) {
    const int d = i % DEPTH;
    f32x4 sc = {0.f, 0.f, 0.f, 0.f};
#pragma unroll
    for (int s = 0; s < 4; ++s) sc = __builtin_amdgcn_mfma_f32_16x16x32_bf16(__builtin_bit_cast(bf16x8, kb[d][s]), qf[s], sc, 0, 0, 0);
    float tmax = fmaxf(fmaxf(sc[0], sc[1]), fmaxf(sc[2], sc[3]));
    tmax = xrow_max(tmax);
    const float mnew = fmaxf(m_run, tmax);
    const float alpha = __builtin_amdgcn_exp2f((m_run - mnew) * SOFT_C);
    const float mc = -mnew * SOFT_C;
    float pr[4];
#pragma unroll
    for (int r = 0; r < 4; ++r) pr[r] = __builtin_amdgcn_exp2f(fmaf(sc[r], SOFT_C, mc));
    l_run = fmaf(l_run, alpha, (pr[0] + pr[1]) + (pr[2] + pr[3]));
    m_run = mnew;
    olo *= alpha; ohi *= alpha;
#pragma unroll
    for (int r = 0; r < 4; ++r) {
      const unsigned pk = cvtpk(pr[r], pr[r]);
      const u32x2 bp = {pk & msk0, pk & msk1};
      const u32x2 vlo = {vb[d][r][0], vb[d][r][1]}, vhi = {vb[d][r][2], vb[d][r][3]};
      olo = __builtin_amdgcn_mfma_f32_16x16x16bf16_1k(__builtin_bit_cast(s16x4, vlo), __builtin_bit_cast(s16x4, bp), olo, 0, 0, 0);
      ohi = __builtin_amdgcn_mfma_f32_16x16x16bf16_1k(__builtin_bit_cast(s16x4, vhi), __builtin_bit_cast(s16x4, bp), ohi, 0, 0, 0);
    }
    __builtin_amdgcn_sched_barrier(0);
    if (i == 6 && tid < 2 * NFF) yg = __builtin_amdgcn_raw_buffer_load_b128(wsr, yoff, 0, AUX_SC1);
    if (i + DEPTH < TPW) ISSUE(d, i + DEPTH);
    __builtin_amdgcn_sched_barrier(0);
  }
#undef ISSUE
  STAMP(3);
  l_run = xrow_sum(l_run);
  {
    float* mo = mrg_o + wave * 512 + (n16 >> 2) * DH + g4 * 32 + (n16 & 3);
#pragma unroll
    for (int r = 0; r < 4; ++r) { mo[r * 8] = olo[r]; mo[r * 8 + 4] = ohi[r]; }
    if (g4 == 0 && (n16 & 3) == 0) { mrg_m[wave * 4 + (n16 >> 2)] = m_run; mrg_l[wave * 4 + (n16 >> 2)] = l_run; }
  }
  {
    unsigned spins = 0;
    const bool act = tid < 2 * NFF;
    while (!__all(!act || (ok24(yg[0], yg[1], t40_lo, t40_hi) & ok24(yg[2], yg[3], t40_lo, t40_hi)))) {
      if (++spins > SPIN_CAP) break;
      __builtin_amdgcn_s_sleep(1);
      asm volatile("" ::: "memory");
      if (act) yg = __builtin_amdgcn_raw_buffer_load_b128(wsr, yoff, 0, AUX_SC1);
    }
    float y0 = act ? val24(yg[0]) : 0.f, y1 = act ? val24(yg[2]) : 0.f;
    y0 += dpp<ROR8>(y0); y0 += dpp<ROR4>(y0); y0 += dpp<ROR2>(y0); y0 = xrow_sum(y0);
    y1 += dpp<ROR8>(y1); y1 += dpp<ROR4>(y1); y1 += dpp<ROR2>(y1); y1 = xrow_sum(y1);
    if (lane < 2) { fin_y[wave * 4 + lane * 2] = y0; fin_y[wave * 4 + lane * 2 + 1] = y1; }
  }
  __syncthreads();
  {
    const int h = tid >> 7;
    float mw[8], M = -INFINITY;
#pragma unroll
    for (int v = 0; v < 8; ++v) { mw[v] = mrg_m[v * 4 + h]; M = fmaxf(M, mw[v]); }
    float o = 0.f, L = 0.f;
#pragma unroll
    for (int v = 0; v < 8; ++v) {
      const float f = __builtin_amdgcn_exp2f((mw[v] - M) * SOFT_C);
      o = fmaf(f, mrg_o[v * 512 + tid], o); L = fmaf(f, mrg_l[v * 4 + h], L);
    }
    const unsigned abase = WS_AP + (unsigned)((w * 4 + h) * AP_STRIDE) * 8;
    __builtin_amdgcn_raw_buffer_store_b64(gr24(f24(o), t40_lo, t40_hi), wsr, abase + (tid & 127) * 8, 0, AUX_SC1);
    if ((tid & 127) < 3) {
      const unsigned mb = __float_as_uint(M);
      const unsigned pay = (tid & 127) == 0 ? (mb >> 8) : (tid & 127) == 1 ? (mb & 0xffu) : f24(L);
      __builtin_amdgcn_raw_buffer_store_b64(gr24(pay, t40_lo, t40_hi), wsr, abase + (128 + (tid & 127)) * 8, 0, AUX_SC1);
    }
  }
  STAMP(4);
  {
    const int pp = tid >> 4, j = tid & 15;
    const int wp = kvh + 8 * (pp >> 4) + 16 * (pp & 15);
    const unsigned abase = WS_AP + (unsigned)((wp * 4 + (idx >> 3)) * AP_STRIDE) * 8;
    const unsigned o1 = abase + (unsigned)((idx & 7) * 16 + j) * 8, o2 = abase + (unsigned)(128 + (j < 3 ? j : 2)) * 8;
    u32x2 ga = __builtin_amdgcn_raw_buffer_load_b64(wsr, o1, 0, AUX_SC1);
    u32x2 gs = __builtin_amdgcn_raw_buffer_load_b64(wsr, o2, 0, AUX_SC1);
    unsigned spins = 0;
    while (!__all(ok24(ga[0], ga[1], t40_lo, t40_hi) & ok24(gs[0], gs[1], t40_lo, t40_hi))) {
      if (++spins > SPIN_CAP) break;
      __builtin_amdgcn_s_sleep(1);
      asm volatile("" ::: "memory");
      ga = __builtin_amdgcn_raw_buffer_load_b64(wsr, o1, 0, AUX_SC1);
      gs = __builtin_amdgcn_raw_buffer_load_b64(wsr, o2, 0, AUX_SC1);
    }
    c_acc[tid] = val24(ga[0]);
    if (j < 3) c_st[pp * 4 + j] = gs[0] & 0x00ffffffu;
  }
  __syncthreads();
  STAMP(5);
  {
    const int b = (lane >> 4) & 1;
    float mp[16], M = -INFINITY;
#pragma unroll
    for (int s = 0; s < 16; ++s) { mp[s] = __uint_as_float((c_st[(b * 16 + s) * 4] << 8) | c_st[(b * 16 + s) * 4 + 1]); M = fmaxf(M, mp[s]); }
    float o = 0.f, L = 0.f;
#pragma unroll
    for (int s = 0; s < 16; ++s) {
      const float f = __builtin_amdgcn_exp2f((mp[s] - M) * SOFT_C);
      o = fmaf(f, c_acc[(b * 16 + s) * 16 + n16], o); L = fmaf(f, val24(c_st[(b * 16 + s) * 4 + 2]), L);
    }
    o = rbf(o / L);
    const float o00 = __shfl(o, frow), o01 = __shfl(o, 8 + frow), o10 = __shfl(o, 16 + frow), o11 = __shfl(o, 24 + frow);
    float sa[8];
#pragma unroll
    for (int j = 0; j < 4; ++j) {
      const float l0 = bf_lo(woa[j]), h0 = bf_hi(woa[j]), l1 = bf_lo(wob[j]), h1 = bf_hi(wob[j]);
      sa[2 * j] = tr32(fmaf(o01, l1, o00 * l0), fmaf(o11, l1, o10 * l0));
      sa[2 * j + 1] = tr32(fmaf(o01, h1, o00 * h0), fmaf(o11, h1, o10 * h0));
    }
#pragma unroll
    for (int j = 0; j < 4; ++j) sa[j] = tr16(sa[j], sa[j + 4]);
    const bool b3 = (lane >> 3) & 1;
    const float s0 = tr8(sa[0], sa[2], b3), s1 = tr8(sa[1], sa[3], b3);
    const unsigned col = (unsigned)((lane >> 5) * DM + wave * 64 + fcc * 8 + ((lane >> 4) & 1) * 4 + (b3 ? 2 : 0));
    __builtin_amdgcn_raw_buffer_store_b128(u32x4{f24(s0) | t40_lo, t40_hi, f24(s1) | t40_lo, t40_hi}, wsr, WS_SP + ((unsigned)w * 1024 + col) * 8, 0, AUX_SC1);
  }
  STAMP(6);
  {
    const unsigned soff = WS_SP + ((unsigned)(tid >> 1) * 1024 + (unsigned)w * 4 + (tid & 1) * 2) * 8;
    u32x4 sg = __builtin_amdgcn_raw_buffer_load_b128(wsr, soff, 0, AUX_SC1);
    unsigned spins = 0;
    while (!__all(ok24(sg[0], sg[1], t40_lo, t40_hi) & ok24(sg[2], sg[3], t40_lo, t40_hi))) {
      if (++spins > SPIN_CAP) break;
      __builtin_amdgcn_s_sleep(1);
      asm volatile("" ::: "memory");
      sg = __builtin_amdgcn_raw_buffer_load_b128(wsr, soff, 0, AUX_SC1);
    }
    float s0 = val24(sg[0]), s1 = val24(sg[2]);
    s0 += dpp<ROR8>(s0); s0 += dpp<ROR4>(s0); s0 += dpp<ROR2>(s0); s0 = xrow_sum(s0);
    s1 += dpp<ROR8>(s1); s1 += dpp<ROR4>(s1); s1 += dpp<ROR2>(s1); s1 = xrow_sum(s1);
    if (lane < 2) { fin_s[wave * 4 + lane * 2] = s0; fin_s[wave * 4 + lane * 2 + 1] = s1; }
  }
  __syncthreads();
  if (tid < 4) {
    float s = 0.f, y = 0.f;
#pragma unroll
    for (int v = 0; v < 8; ++v) { s += fin_s[v * 4 + tid]; y += fin_y[v * 4 + tid]; }
    s = rbf(s); y = rbf(y);
    const float g = rbf(y / (1.f + __expf(-y)));
    const float xv = __uint_as_float((unsigned)p.x[w * 4 + tid] << 16);
    const float r = xv + rbf(s * g);
    p.out[w * 4 + tid] = (unsigned short)(cvtpk(r, r) & 0xffffu);
    if (w == 0 && tid == 0) __hip_atomic_store(reinterpret_cast<u64*>(p.ws + WS_EPOCH), tag, __ATOMIC_RELAXED, __HIP_MEMORY_SCOPE_AGENT);
  }
  STAMP(7);
}

}

extern "C" void kernel_launch(void* const* d_in, const int* in_sizes, int n_in, void* d_out, int out_size, void* d_ws, size_t ws_size,
                              hipStream_t stream) {
  Params p;
  p.x = (const unsigned short*)d_in[0];
  p.k = (const unsigned short*)d_in[1];
  p.v = (const unsigned short*)d_in[2];
  p.wq = (const unsigned short*)d_in[3];
  p.wo = (const unsigned short*)d_in[4];
  p.w1 = (const unsigned short*)d_in[6];
  p.w2 = (const unsigned short*)d_in[7];
  p.out = (unsigned short*)d_out;
  p.ws = (unsigned char*)d_ws;
  hipLaunchKernelGGL(fused_decode_kernel, dim3(NWG), dim3(NTHR), 0, stream, p);
}
```

```cpp
#include <hip/hip_runtime.h>
#include <hip/hip_bf16.h>
#include <stdint.h>

typedef unsigned u32x4 __attribute__((ext_vector_type(4)));
typedef unsigned u32x2 __attribute__((ext_vector_type(2)));
typedef float f32x4 __attribute__((ext_vector_type(4)));
typedef float f32x2 __attribute__((ext_vector_type(2)));
typedef __bf16 bf16x2 __attribute__((ext_vector_type(2)));
typedef __bf16 bf16x8 __attribute__((ext_vector_type(8)));
typedef short s16x4 __attribute__((ext_vector_type(4)));
typedef unsigned long long u64;

namespace {

constexpr int NWG = 256, NTHR = 512, NWAVE = 8;
constexpr int SK = 32768, DH = 128, DM = 512, DFF = 1792, NQ = 4096;
constexpr int DEPTH = 6;
constexpr int NFULL = 4, REM = 2;
static_assert(4 * (NFULL * DEPTH + REM) + 4 * DEPTH == 128, "128 tiles of 16 keys per workgroup");
constexpr int NFF = DFF / 8;
constexpr unsigned WS_EPOCH = 0;
constexpr unsigned WS_QG = 4096;
constexpr unsigned WS_HP = WS_QG + 4 * 2 * NQ * 8;
constexpr unsigned WS_AP = WS_HP + 8 * 28 * 128 * 8;
constexpr unsigned WS_ST = WS_AP + NWG * 4 * 128 * 8;
constexpr unsigned WS_YP = WS_ST + 8 * 4 * 32 * 4 * 8;
constexpr unsigned WS_SP = WS_YP + 28 * 1024 * 8;
constexpr unsigned WS_END = WS_SP + 64 * 1024 * 8;
constexpr unsigned WS_DBG = 8u << 20;
constexpr unsigned SPIN_CAP = 1u << 16;
constexpr int AUX_NT = 2, AUX_SC1 = 16;
constexpr float SOFT_C = 0.08838834764831845f * 1.4426950408889634f;

__device__ __forceinline__ float bf_lo(unsigned u) { return __uint_as_float(u << 16); }
__device__ __forceinline__ float bf_hi(unsigned u) { return __uint_as_float(u & 0xffff0000u); }
__device__ __forceinline__ unsigned cvtpk(float lo, float hi) {
  return __builtin_bit_cast(unsigned, __builtin_convertvector(f32x2{lo, hi}, bf16x2));
}
__device__ __forceinline__ float rbf(float x) { return bf_lo(cvtpk(x, x)); }
template <int CTRL> __device__ __forceinline__ float dpp(float x) {
  return __builtin_bit_cast(float, __builtin_amdgcn_mov_dpp(__builtin_bit_cast(int, x), CTRL, 0xf, 0xf, true));
}
constexpr int ROR1 = 0x121, ROR2 = 0x122, ROR4 = 0x124, ROR8 = 0x128, XOR1 = 0xB1, XOR2 = 0x4E, XOR7 = 0x141;
__device__ __forceinline__ float xrow_max(float x) {
  auto s = __builtin_amdgcn_permlane16_swap(__float_as_uint(x), __float_as_uint(x), false, false);
  x = fmaxf(__uint_as_float(s[0]), __uint_as_float(s[1]));
  auto t = __builtin_amdgcn_permlane32_swap(__float_as_uint(x), __float_as_uint(x), false, false);
  return fmaxf(__uint_as_float(t[0]), __uint_as_float(t[1]));
}
__device__ __forceinline__ float xrow_sum(float x) {
  auto s = __builtin_amdgcn_permlane16_swap(__float_as_uint(x), __float_as_uint(x), false, false);
  x = __uint_as_float(s[0]) + __uint_as_float(s[1]);
  auto t = __builtin_amdgcn_permlane32_swap(__float_as_uint(x), __float_as_uint(x), false, false);
  return __uint_as_float(t[0]) + __uint_as_float(t[1]);
}
__device__ __forceinline__ float wave_sum(float x) {
  x += dpp<ROR8>(x); x += dpp<ROR4>(x); x += dpp<ROR2>(x); x += dpp<ROR1>(x);
  return xrow_sum(x);
}
__device__ __forceinline__ float tr32(float a, float b) {
  auto t = __builtin_amdgcn_permlane32_swap(__float_as_uint(a), __float_as_uint(b), false, false);
  return __uint_as_float(t[0]) + __uint_as_float(t[1]);
}
__device__ __forceinline__ float tr16(float a, float b) {
  auto t = __builtin_amdgcn_permlane16_swap(__float_as_uint(a), __float_as_uint(b), false, false);
  return __uint_as_float(t[0]) + __uint_as_float(t[1]);
}
__device__ __forceinline__ float tr8(float a, float b, bool bit3) {
  const float keep = bit3 ? b : a, send = bit3 ? a : b;
  return keep + dpp<ROR8>(send);
}
__device__ __forceinline__ float tr4(float a, float b, bool bit2) {
  const float keep = bit2 ? b : a, send = bit2 ? a : b;
  return keep + dpp<XOR7>(send);
}
__device__ __forceinline__ unsigned f24(float x) { return (__float_as_uint(x) + 0x80u) >> 8; }
__device__ __forceinline__ u32x2 gr24(unsigned payload24, unsigned tlo8, unsigned thi) { return u32x2{payload24 | tlo8, thi}; }
__device__ __forceinline__ bool ok24(unsigned lo, unsigned hi, unsigned tlo8, unsigned thi) { return ((lo & 0xff000000u) == tlo8) & (hi == thi); }
__device__ __forceinline__ float val24(unsigned lo) { return __uint_as_float(lo << 8); }

struct Params {
  const unsigned short* x; const unsigned short* k; const unsigned short* v; const unsigned short* wq; const unsigned short* wo;
  const unsigned short* w1; const unsigned short* w2; unsigned short* out; unsigned char* ws;
};

#define START_WIN 2
#define WARM_LIVE0 0
#define STAMP(i) do {} while (0)
#define LDW1(p_) LDNT(p_)
#define Q_DELAY 32
#define EARLY_GO 3
#define YP_DELAY 1
#define LDNT(p_) __builtin_nontemporal_load(reinterpret_cast<const u32x4*>(p_))

__device__ __forceinline__ void layernorm_to_lds(const u32x4 xa, const u32x4 xb, float* dst, int lane) {
  float e0[8], e1[8];
#pragma unroll
  for (int j = 0; j < 4; ++j) { e0[2 * j] = bf_lo(xa[j]); e0[2 * j + 1] = bf_hi(xa[j]); e1[2 * j] = bf_lo(xb[j]); e1[2 * j + 1] = bf_hi(xb[j]); }
  float s0 = 0.f, s1 = 0.f;
#pragma unroll
  for (int j = 0; j < 8; ++j) { s0 += e0[j]; s1 += e1[j]; }
  s0 = wave_sum(s0) * (1.f / DM); s1 = wave_sum(s1) * (1.f / DM);
  float v0 = 0.f, v1 = 0.f;
#pragma unroll
  for (int j = 0; j < 8; ++j) { const float a = e0[j] - s0, b = e1[j] - s1; v0 = fmaf(a, a, v0); v1 = fmaf(b, b, v1); }
  v0 = wave_sum(v0) * (1.f / DM); v1 = wave_sum(v1) * (1.f / DM);
  const float m0 = rbf(s0), m1 = rbf(s1);
  const float d0 = 1.f / rbf(sqrtf(rbf(rbf(v0) + 1e-5f))), d1 = 1.f / rbf(sqrtf(rbf(rbf(v1) + 1e-5f)));
#pragma unroll
  for (int j = 0; j < 8; j += 2)
    *reinterpret_cast<f32x4*>(dst + (8 * lane + j) * 2) =
        f32x4{rbf(rbf(e0[j] - m0) * d0), rbf(rbf(e1[j] - m1) * d1), rbf(rbf(e0[j + 1] - m0) * d0), rbf(rbf(e1[j + 1] - m1) * d1)};
}

__device__ __forceinline__ bool ok24x2(const u32x4 g, unsigned tlo8, unsigned thi) { return ok24(g[0], g[1], tlo8, thi) & ok24(g[2], g[3], tlo8, thi); }
__device__ __forceinline__ u32x4 gr24x2(float a, float b, unsigned tlo8, unsigned thi) { return u32x4{f24(a) | tlo8, thi, f24(b) | tlo8, thi}; }

__global__ void __launch_bounds__(NTHR, 2) fused_decode_kernel(Params p) {
  __shared__ __attribute__((aligned(16))) float lds[13312];
  float* xn_s = lds;
  float* red_q = lds + 1024;
  float* mrg_m = lds + 1536;
  float* mrg_l = lds + 1568;
  float* c_scl = lds + 1600;
  float* fin_s = lds + 1632;
  float* tr_h = lds + 1760;
  float* h_s = lds + 1888;
  float* g_s = lds + 2016;
  unsigned* q_s = reinterpret_cast<unsigned*>(lds + 2048);
  u64* flag_w = reinterpret_cast<u64*>(lds + 2304);
  float* c_acc = lds + 2560;
  float* mrg_o = lds + 4608;
  u32x4* wo_s = reinterpret_cast<u32x4*>(lds + 8704);

  const int w = blockIdx.x, tid = threadIdx.x, lane = tid & 63;
  const int wave = __builtin_amdgcn_readfirstlane(tid >> 6);
  const int kvh = w & 7, bat = (w >> 3) & 1, split = w >> 4, idx = w >> 3;
  const int n16 = lane & 15, g4 = lane >> 4, frow = lane >> 3, fcc = lane & 7;
  STAMP(0);

  const __amdgpu_buffer_rsrc_t wsr = __builtin_amdgcn_make_buffer_rsrc((void*)p.ws, 0, WS_END, 0x00020000);
#define LOAD_EPOCH() __hip_atomic_load(reinterpret_cast<const u64*>(p.ws + WS_EPOCH), __ATOMIC_RELAXED, __HIP_MEMORY_SCOPE_AGENT)
#define WG_BARRIER() do { __builtin_amdgcn_sched_barrier(0); asm volatile("s_waitcnt lgkmcnt(0)\n\ts_barrier" ::: "memory"); __builtin_amdgcn_sched_barrier(0); } while (0)
#define LDG(off_) __builtin_amdgcn_raw_buffer_load_b128(wsr, (off_), 0, AUX_SC1)
#define STG(v_, off_) __builtin_amdgcn_raw_buffer_store_b128((v_), wsr, (off_), 0, AUX_SC1)
  const bool streamer = wave >= 4;

#define SET_FLAG(i_, t_) __hip_atomic_store(&flag_w[i_], (t_), __ATOMIC_RELEASE, __HIP_MEMORY_SCOPE_WORKGROUP)
#define GET_FLAG(i_) __hip_atomic_load(&flag_w[i_], __ATOMIC_ACQUIRE, __HIP_MEMORY_SCOPE_WORKGROUP)
  const __amdgpu_buffer_rsrc_t kr = __builtin_amdgcn_make_buffer_rsrc((void*)p.k, 0, 2u * 8 * SK * DH * 2, 0x00020000);
  const __amdgpu_buffer_rsrc_t vr = __builtin_amdgcn_make_buffer_rsrc((void*)p.v, 0, 2u * 8 * SK * DH * 2, 0x00020000);
  const unsigned kvoff = (unsigned)(((n16 & 3) * 4 + (n16 >> 2)) * 256 + g4 * 16);
  const unsigned vvoff = (unsigned)lane * 16;
  const unsigned sbase = (unsigned)((bat * 8 + kvh) * SK + split * 2048 + ((streamer ? 4 * DEPTH : 0) + (wave & 3)) * 16) * (DH * 2);
  constexpr unsigned TSTEP = 4 * 16 * DH * 2;
  u32x4 kb[DEPTH][4], vb[DEPTH][4];
#define ISSUE(d_, so_) do { \
    _Pragma("unroll") for (int s_ = 0; s_ < 4; ++s_) kb[d_][s_] = __builtin_amdgcn_raw_buffer_load_b128(kr, kvoff + s_ * 64, (so_), AUX_NT); \
    _Pragma("unroll") for (int s_ = 0; s_ < 4; ++s_) vb[d_][s_] = __builtin_amdgcn_raw_buffer_load_b128(vr, vvoff + s_ * 1024, (so_), AUX_NT); \
    __builtin_amdgcn_sched_barrier(0); } while (0)
  u64 tag;
  if (__builtin_expect(streamer, 1)) {
    STAMP(9);
    ISSUE(0, sbase);
    tag = LOAD_EPOCH() + 1;
    __builtin_amdgcn_sched_barrier(0);
#pragma unroll
    for (int d = 1; d < DEPTH; ++d) ISSUE(d, sbase + d * TSTEP);
  } else {
    const u32x4 xa = *reinterpret_cast<const u32x4*>(p.x + 8 * lane);
    const u32x4 xb = *reinterpret_cast<const u32x4*>(p.x + DM + 8 * lane);
    tag = LOAD_EPOCH() + 1;
    __builtin_amdgcn_sched_barrier(0);
    const int qrow = (w >> 6) * 128 + wave * 32 + frow;
    const unsigned short* wqp = p.wq + (size_t)qrow * NQ + (w & 63) * 64 + fcc * 8;
    u32x4 wv[4];
#pragma unroll
    for (int k = 0; k < 4; ++k) wv[k] = LDNT(wqp + (size_t)k * 8 * NQ);
    __builtin_amdgcn_sched_barrier(0);
    layernorm_to_lds(xa, xb, xn_s, lane);
    const unsigned t40_lo = (unsigned)(tag << 24), t40_hi = (unsigned)(tag >> 8);
    const bool b3 = (lane >> 3) & 1;
    {
      float qa[16];
#pragma unroll
      for (int j = 0; j < 16; ++j) qa[j] = 0.f;
#pragma unroll
      for (int k = 0; k < 4; ++k) {
        const f32x2 xv = *reinterpret_cast<const f32x2*>(xn_s + (qrow + 8 * k) * 2);
#pragma unroll
        for (int j = 0; j < 4; ++j) {
          const float l0 = bf_lo(wv[k][j]), h0 = bf_hi(wv[k][j]);
          qa[2 * j] = fmaf(xv[0], l0, qa[2 * j]); qa[2 * j + 1] = fmaf(xv[0], h0, qa[2 * j + 1]);
          qa[8 + 2 * j] = fmaf(xv[1], l0, qa[8 + 2 * j]); qa[8 + 2 * j + 1] = fmaf(xv[1], h0, qa[8 + 2 * j + 1]);
        }
      }
#pragma unroll
      for (int j = 0; j < 8; ++j) qa[j] = tr32(qa[j], qa[j + 8]);
#pragma unroll
      for (int j = 0; j < 4; ++j) qa[j] = tr16(qa[j], qa[j + 4]);
      *reinterpret_cast<f32x2*>(red_q + wave * 128 + (lane >> 5) * 64 + fcc * 8 + ((lane >> 3) & 3) * 2) = f32x2{tr8(qa[0], qa[2], b3), tr8(qa[1], qa[3], b3)};
      SET_FLAG(wave, tag);
    }
    u32x4 w1v[8];
    const bool has_f1 = w < NFF;
    const int f1row = (w / 28) * 64 + frow;
    if (wave == 0) {
      while (!__all(GET_FLAG(lane & 3) == tag)) __builtin_amdgcn_s_sleep(0);
      f32x2 t = *reinterpret_cast<const f32x2*>(red_q + 2 * lane);
#pragma unroll
      for (int v = 1; v < 4; ++v) t += *reinterpret_cast<const f32x2*>(red_q + v * 128 + 2 * lane);
      STG(gr24x2(t[0], t[1], t40_lo, t40_hi), WS_QG + (unsigned)(((w >> 6) * 2 + (lane >> 5)) * NQ + (w & 63) * 64 + (lane & 31) * 2) * 8);
      STAMP(1);
    } else if (wave == 1 && has_f1) {
#pragma unroll
      for (int k = 0; k < 8; ++k) w1v[k] = LDNT(p.w1 + (size_t)(f1row + 8 * k) * DFF + (w % 28) * 64 + fcc * 8);
    }
    __builtin_amdgcn_sched_barrier(0);
    {
      const unsigned qoff = WS_QG + (unsigned)(bat * NQ + kvh * 512 + wave * 128 + 2 * lane) * 8;
      u32x4 qg[4];
      unsigned spins = 0;
      __builtin_amdgcn_s_sleep(Q_DELAY);
#pragma unroll
      for (int k = 0; k < 4; ++k) qg[k] = LDG(qoff + k * (2 * NQ * 8));
      for (;;) {
        const bool k0 = __all(ok24x2(qg[0], t40_lo, t40_hi)), k1 = __all(ok24x2(qg[1], t40_lo, t40_hi));
        const bool k2 = __all(ok24x2(qg[2], t40_lo, t40_hi)), k3 = __all(ok24x2(qg[3], t40_lo, t40_hi));
        if ((k0 & k1 & k2 & k3) || ++spins >= SPIN_CAP) break;
        __builtin_amdgcn_s_sleep(8);
        asm volatile("" ::: "memory");
        if (!k0) qg[0] = LDG(qoff);
        if (!k1) qg[1] = LDG(qoff + 1 * (2 * NQ * 8));
        if (!k2) qg[2] = LDG(qoff + 2 * (2 * NQ * 8));
        if (!k3) qg[3] = LDG(qoff + 3 * (2 * NQ * 8));
      }
      q_s[wave * 64 + lane] = cvtpk((val24(qg[0][0]) + val24(qg[1][0])) + (val24(qg[2][0]) + val24(qg[3][0])),
                                    (val24(qg[0][2]) + val24(qg[1][2])) + (val24(qg[2][2]) + val24(qg[3][2])));
      SET_FLAG(4 + wave, tag);
      STAMP(11);
    }
    if (wave == 1 && has_f1) {
      float ha[16];
#pragma unroll
      for (int j = 0; j < 16; ++j) ha[j] = 0.f;
#pragma unroll
      for (int k = 0; k < 8; ++k) {
        const f32x2 xv = *reinterpret_cast<const f32x2*>(xn_s + (f1row + 8 * k) * 2);
#pragma unroll
        for (int j = 0; j < 4; ++j) {
          const float l0 = bf_lo(w1v[k][j]), h0 = bf_hi(w1v[k][j]);
          ha[2 * j] = fmaf(xv[0], l0, ha[2 * j]); ha[2 * j + 1] = fmaf(xv[0], h0, ha[2 * j + 1]);
          ha[8 + 2 * j] = fmaf(xv[1], l0, ha[8 + 2 * j]); ha[8 + 2 * j + 1] = fmaf(xv[1], h0, ha[8 + 2 * j + 1]);
        }
      }
#pragma unroll
      for (int j = 0; j < 8; ++j) ha[j] = tr32(ha[j], ha[j + 8]);
#pragma unroll
      for (int j = 0; j < 4; ++j) ha[j] = tr16(ha[j], ha[j + 4]);
      { const int u = fcc * 8 + (frow & 3) * 2;
        tr_h[u * 2 + (lane >> 5)] = tr8(ha[0], ha[2], b3); tr_h[u * 2 + 2 + (lane >> 5)] = tr8(ha[1], ha[3], b3); }
      const f32x2 t = *reinterpret_cast<const f32x2*>(tr_h + 2 * lane);
      STG(gr24x2(t[0], t[1], t40_lo, t40_hi), WS_HP + (unsigned)(w * 128 + 2 * lane) * 8);
      STAMP(10);
    } else if (wave == 2) {
      u32x4 t[16];
#pragma unroll
      for (int k = 0; k < 16; ++k) t[k] = LDNT(p.wo + (size_t)(kvh * 512 + (idx >> 2) * 64 + k * 4 + g4) * DM + (idx & 3) * 128 + n16 * 8);
#pragma unroll
      for (int k = 0; k < 16; ++k) wo_s[(k * 4 + g4) * 17 + n16] = t[k];
    }
    __builtin_amdgcn_sched_barrier(0);
#pragma unroll
    for (int d = 0; d < DEPTH; ++d) ISSUE(d, sbase + d * TSTEP);
  }

  {
    const unsigned msk0 = (n16 & 3) == 0 ? 0x0000ffffu : (n16 & 3) == 1 ? 0xffff0000u : 0u;
    const unsigned msk1 = (n16 & 3) == 2 ? 0x0000ffffu : (n16 & 3) == 3 ? 0xffff0000u : 0u;
    float m_run = -INFINITY, l_run = 0.f;
    f32x4 olo = {0.f, 0.f, 0.f, 0.f}, ohi = {0.f, 0.f, 0.f, 0.f};
    while (!__all(GET_FLAG(4 + (lane & 3)) == tag)) __builtin_amdgcn_s_sleep(1);
    bf16x8 qf[4];
#pragma unroll
    for (int s = 0; s < 4; ++s) qf[s] = *reinterpret_cast<const bf16x8*>(q_s + ((n16 >> 2) * DH + s * 32 + g4 * 8) / 2);
    STAMP(2);
#define TILE(d) do { \
      f32x4 sc = {0.f, 0.f, 0.f, 0.f}; \
      _Pragma("unroll") for (int s = 0; s < 4; ++s) sc = __builtin_amdgcn_mfma_f32_16x16x32_bf16(__builtin_bit_cast(bf16x8, kb[d][s]), qf[s], sc, 0, 0, 0); \
      float tmax = fmaxf(fmaxf(sc[0], sc[1]), fmaxf(sc[2], sc[3])); \
      tmax = xrow_max(tmax); \
      const float mnew = fmaxf(m_run, tmax); \
      const float alpha = __builtin_amdgcn_exp2f((m_run - mnew) * SOFT_C); \
      const float mc = -mnew * SOFT_C; \
      float pr[4]; \
      _Pragma("unroll") for (int r = 0; r < 4; ++r) pr[r] = __builtin_amdgcn_exp2f(fmaf(sc[r], SOFT_C, mc)); \
      l_run = fmaf(l_run, alpha, (pr[0] + pr[1]) + (pr[2] + pr[3])); \
      m_run = mnew; \
      olo *= alpha; ohi *= alpha; \
      _Pragma("unroll") for (int r = 0; r < 4; ++r) { \
        const unsigned pk = cvtpk(pr[r], pr[r]); \
        const u32x2 bp = {pk & msk0, pk & msk1}; \
        const u32x2 vlo = {vb[d][r][0], vb[d][r][1]}, vhi = {vb[d][r][2], vb[d][r][3]}; \
        olo = __builtin_amdgcn_mfma_f32_16x16x16bf16_1k(__builtin_bit_cast(s16x4, vlo), __builtin_bit_cast(s16x4, bp), olo, 0, 0, 0); \
        ohi = __builtin_amdgcn_mfma_f32_16x16x16bf16_1k(__builtin_bit_cast(s16x4, vhi), __builtin_bit_cast(s16x4, bp), ohi, 0, 0, 0); \
      } \
      __builtin_amdgcn_sched_barrier(0); } while (0)
    if (streamer) {
#pragma unroll
      for (int d = 0; d < REM; ++d) { TILE(d); ISSUE(d, sbase + (unsigned)(DEPTH + d) * TSTEP); }
#pragma unroll 1
      for (int it = 0; it < NFULL - 1; ++it) {
        const unsigned snext = sbase + (unsigned)(REM + (it + 1) * DEPTH) * TSTEP;
#pragma unroll
        for (int d = 0; d < DEPTH; ++d) {
          TILE((d + REM) % DEPTH);
          ISSUE((d + REM) % DEPTH, snext + d * TSTEP);
        }
      }
#pragma unroll
      for (int d = 0; d < DEPTH; ++d) TILE((d + REM) % DEPTH);
    } else {
#pragma unroll
      for (int d = 0; d < DEPTH; ++d) TILE(d);
    }
#undef TILE
#undef ISSUE
    STAMP(3);
    l_run = xrow_sum(l_run);
    float* mo = mrg_o + wave * 512 + (n16 >> 2) * DH + g4 * 32 + (n16 & 3);
#pragma unroll
    for (int r = 0; r < 4; ++r) { mo[r * 8] = olo[r]; mo[r * 8 + 4] = ohi[r]; }
    if (g4 == 0 && (n16 & 3) == 0) { mrg_m[wave * 4 + (n16 >> 2)] = m_run; mrg_l[wave * 4 + (n16 >> 2)] = l_run; }
  }
  const unsigned t40_lo = (unsigned)(tag << 24), t40_hi = (unsigned)(tag >> 8);
  if (wave == 3) {
    const bool b3 = (lane >> 3) & 1;
    if (w < NFF) {
      u32x4 w2v[8];
#pragma unroll
      for (int k = 0; k < 8; ++k) w2v[k] = LDNT(p.w2 + (size_t)((w >> 3) * 64 + frow + 8 * k) * DM + (w & 7) * 64 + fcc * 8);
      const unsigned hoff = WS_HP + (unsigned)((w >> 3) * 128 + 2 * lane) * 8;
      u32x4 hg[8];
      unsigned spins = 0;
      for (;;) {
        bool ok = true;
#pragma unroll
        for (int k = 0; k < 8; ++k) hg[k] = LDG(hoff + k * (28 * 128 * 8));
#pragma unroll
        for (int k = 0; k < 8; ++k) ok &= ok24x2(hg[k], t40_lo, t40_hi);
        if (__all(ok) || ++spins >= SPIN_CAP) break;
        __builtin_amdgcn_s_sleep(32);
        asm volatile("" ::: "memory");
      }
      float h0 = 0.f, h1 = 0.f;
#pragma unroll
      for (int k = 0; k < 8; ++k) { h0 += val24(hg[k][0]); h1 += val24(hg[k][2]); }
      *reinterpret_cast<f32x2*>(h_s + 2 * lane) = f32x2{fmaxf(rbf(h0), 0.f), fmaxf(rbf(h1), 0.f)};
      float ya[16];
#pragma unroll
      for (int j = 0; j < 16; ++j) ya[j] = 0.f;
#pragma unroll
      for (int k = 0; k < 8; ++k) {
        const f32x2 hv = *reinterpret_cast<const f32x2*>(h_s + (frow + 8 * k) * 2);
#pragma unroll
        for (int j = 0; j < 4; ++j) {
          const float l0 = bf_lo(w2v[k][j]), hh = bf_hi(w2v[k][j]);
          ya[2 * j] = fmaf(hv[0], l0, ya[2 * j]); ya[2 * j + 1] = fmaf(hv[0], hh, ya[2 * j + 1]);
          ya[8 + 2 * j] = fmaf(hv[1], l0, ya[8 + 2 * j]); ya[8 + 2 * j + 1] = fmaf(hv[1], hh, ya[8 + 2 * j + 1]);
        }
      }
#pragma unroll
      for (int j = 0; j < 8; ++j) ya[j] = tr32(ya[j], ya[j + 8]);
#pragma unroll
      for (int j = 0; j < 4; ++j) ya[j] = tr16(ya[j], ya[j + 4]);
      asm volatile("" ::: "memory");
      *reinterpret_cast<f32x2*>(h_s + ((fcc >> 1) * 2 + (lane >> 5)) * 16 + ((frow & 3) * 2 + (fcc & 1)) * 2) = f32x2{tr8(ya[0], ya[2], b3), tr8(ya[1], ya[3], b3)};
      const f32x2 t = *reinterpret_cast<const f32x2*>(h_s + 2 * lane);
      STG(gr24x2(t[0], t[1], t40_lo, t40_hi), WS_YP + (unsigned)((w >> 3) * 1024 + (w & 7) * 128 + 2 * lane) * 8);
    }
    STAMP(8);
    if (w < 64) {
      const unsigned yoff = WS_YP + (unsigned)(frow * 1024 + w * 16 + fcc * 2) * 8;
      u32x4 yg[4];
      unsigned spins = 0;
      for (;;) {
        __builtin_amdgcn_s_sleep(64);
        bool ok = true;
#pragma unroll
        for (int k = 0; k < 4; ++k) yg[k] = (k < 3 || lane < 32) ? LDG(yoff + k * 8 * 8192) : u32x4{t40_lo, t40_hi, t40_lo, t40_hi};
#pragma unroll
        for (int k = 0; k < 4; ++k) ok &= ok24x2(yg[k], t40_lo, t40_hi);
        if (__all(ok) || ++spins >= SPIN_CAP) break;
        asm volatile("" ::: "memory");
      }
      float y0 = (val24(yg[0][0]) + val24(yg[1][0])) + (val24(yg[2][0]) + val24(yg[3][0]));
      float y1 = (val24(yg[0][2]) + val24(yg[1][2])) + (val24(yg[2][2]) + val24(yg[3][2]));
      y0 += dpp<ROR8>(y0); y0 = rbf(xrow_sum(y0));
      y1 += dpp<ROR8>(y1); y1 = rbf(xrow_sum(y1));
      if (lane < 8) *reinterpret_cast<f32x2*>(g_s + lane * 2) = f32x2{rbf(y0 / (1.f + __expf(-y0))), rbf(y1 / (1.f + __expf(-y1)))};
    }
    STAMP(12);
  }
  asm volatile("" :: "v"(t40_lo), "v"(t40_hi));
  __syncthreads();
  {
    const int h = tid >> 7;
    float mw[8], M = -INFINITY;
#pragma unroll
    for (int v = 0; v < 8; ++v) { mw[v] = mrg_m[v * 4 + h]; M = fmaxf(M, mw[v]); }
    float o = 0.f, L = 0.f;
#pragma unroll
    for (int v = 0; v < 8; ++v) {
      const float f = __builtin_amdgcn_exp2f((mw[v] - M) * SOFT_C);
      o = fmaf(f, mrg_o[v * 512 + tid], o); L = fmaf(f, mrg_l[v * 4 + h], L);
    }
    __builtin_amdgcn_raw_buffer_store_b64(gr24(f24(o), t40_lo, t40_hi), wsr, WS_AP + (unsigned)(w * 512 + tid) * 8, 0, AUX_SC1);
    if ((tid & 127) < 2) {
      const unsigned mb = __float_as_uint(M);
      const unsigned p0 = (tid & 1) ? f24(L) : (mb >> 8), p1 = (tid & 1) ? 0u : (mb & 0xffu);
      STG((u32x4{p0 | t40_lo, t40_hi, p1 | t40_lo, t40_hi}), WS_ST + (unsigned)(((kvh * 4 + h) * 32 + bat * 16 + split) * 4 + 2 * (tid & 1)) * 8);
    }
  }
  STAMP(4);
  float wof[16];
  {
    const u32x4 woa = wo_s[(lane >> 1) * 17 + wave * 2 + (lane & 1)], wob = wo_s[(32 + (lane >> 1)) * 17 + wave * 2 + (lane & 1)];
#pragma unroll
    for (int j = 0; j < 4; ++j) { wof[2 * j] = bf_lo(woa[j]); wof[2 * j + 1] = bf_hi(woa[j]); wof[8 + 2 * j] = bf_lo(wob[j]); wof[8 + 2 * j + 1] = bf_hi(wob[j]); }
  }
  {
    if (wave == 0) {
      const unsigned o2 = WS_ST + (unsigned)((kvh * 4 + (idx >> 3)) * 128 + lane * 2) * 8;
      u32x4 gs;
      unsigned spins = 0;
      for (;;) {
        gs = LDG(o2);
        const int missing = __popcll(__ballot(!ok24x2(gs, t40_lo, t40_hi)));
        if (missing <= 2 * EARLY_GO) SET_FLAG(8, tag);
        if (missing == 0 || ++spins >= SPIN_CAP) break;
        asm volatile("" ::: "memory");
      }
      SET_FLAG(8, tag);
      const float mine = __uint_as_float((gs[0] << 8) | ((lane & 1) ? 0u : (gs[2] & 0xffu)));
      const float other = dpp<XOR1>(mine);
      const float m = (lane & 1) ? other : mine, l = (lane & 1) ? mine : other;
      float M = m; M = fmaxf(M, dpp<XOR2>(M)); M = fmaxf(M, dpp<XOR7>(M)); M = fmaxf(M, dpp<ROR8>(M));
      { auto t = __builtin_amdgcn_permlane16_swap(__float_as_uint(M), __float_as_uint(M), false, false); M = fmaxf(__uint_as_float(t[0]), __uint_as_float(t[1])); }
      const float f = __builtin_amdgcn_exp2f((m - M) * SOFT_C);
      float L = f * l; L += dpp<XOR2>(L); L += dpp<XOR7>(L); L += dpp<ROR8>(L); L = tr16(L, L);
      if ((lane & 1) == 0) c_scl[lane >> 1] = f / L;
    } else {
      while (GET_FLAG(8) != tag) __builtin_amdgcn_s_sleep(0);
    }
    const int pp0 = wave * 4 + (lane >> 5), pp1 = pp0 + 2;
    const unsigned o1 = WS_AP + (unsigned)((kvh + 8 * (pp0 >> 4) + 16 * (pp0 & 15)) * 512 + (idx >> 2) * 64 + (lane & 31) * 2) * 8;
    u32x4 ga, gb;
    unsigned spins = 0;
    for (;;) {
      ga = LDG(o1); gb = LDG(o1 + 2 * 16 * 512 * 8);
      if (__all(ok24x2(ga, t40_lo, t40_hi) & ok24x2(gb, t40_lo, t40_hi)) || ++spins >= SPIN_CAP) break;
      asm volatile("" ::: "memory");
    }
    *reinterpret_cast<f32x2*>(c_acc + pp0 * 64 + (lane & 31) * 2) = f32x2{val24(ga[0]), val24(ga[2])};
    *reinterpret_cast<f32x2*>(c_acc + pp1 * 64 + (lane & 31) * 2) = f32x2{val24(gb[0]), val24(gb[2])};
  }
  __syncthreads();
  STAMP(5);
  {
    float o0 = 0.f, o1 = 0.f;
#pragma unroll
    for (int s4 = 0; s4 < 4; ++s4) {
      const f32x4 sa = *reinterpret_cast<const f32x4*>(c_scl + s4 * 4), sb = *reinterpret_cast<const f32x4*>(c_scl + 16 + s4 * 4);
#pragma unroll
      for (int j = 0; j < 4; ++j) { o0 = fmaf(sa[j], c_acc[(s4 * 4 + j) * 64 + lane], o0); o1 = fmaf(sb[j], c_acc[(16 + s4 * 4 + j) * 64 + lane], o1); }
    }
    o0 = rbf(o0); o1 = rbf(o1);
    const float o00 = __shfl(o0, lane >> 1), o01 = __shfl(o0, 32 + (lane >> 1)), o10 = __shfl(o1, lane >> 1), o11 = __shfl(o1, 32 + (lane >> 1));
    float sa[8];
#pragma unroll
    for (int j = 0; j < 8; ++j) sa[j] = tr32(fmaf(o01, wof[8 + j], o00 * wof[j]), fmaf(o11, wof[8 + j], o10 * wof[j]));
#pragma unroll
    for (int j = 0; j < 4; ++j) sa[j] = tr16(sa[j], sa[j + 4]);
    const bool b1 = (lane >> 1) & 1;
    float r0 = (b1 ? sa[2] : sa[0]) + dpp<XOR2>(b1 ? sa[0] : sa[2]), r1 = (b1 ? sa[3] : sa[1]) + dpp<XOR2>(b1 ? sa[1] : sa[3]);
    r0 += dpp<ROR4>(r0); r0 += dpp<ROR8>(r0);
    r1 += dpp<ROR4>(r1); r1 += dpp<ROR8>(r1);
    if ((lane & 12) == 0)
      STG(gr24x2(r0, r1, t40_lo, t40_hi),
          WS_SP + (unsigned)((kvh * 8 + (idx >> 2)) * 1024 + (((idx & 3) * 8 + wave) * 2 + (lane >> 5)) * 16 + (((lane >> 4) & 1) * 4 + (lane & 3)) * 2) * 8);
  }
  STAMP(6);
  if (w < 64) {
    const unsigned soff = WS_SP + (unsigned)((wave * 8 + frow) * 1024 + w * 16 + fcc * 2) * 8;
    const int oidx = (w & 1) * DM + (w >> 1) * 16 + (fcc & 1) * 8 + (fcc >> 2) * 4 + ((fcc >> 1) & 1) * 2;
    const unsigned xr = *reinterpret_cast<const unsigned*>(p.x + oidx);
    u32x4 sg;
    unsigned spins = 0;
    for (;;) {
      sg = LDG(soff);
      if (__all(ok24x2(sg, t40_lo, t40_hi)) || ++spins >= SPIN_CAP) break;
      asm volatile("" ::: "memory");
    }
    float s0 = val24(sg[0]), s1 = val24(sg[2]);
    s0 += dpp<ROR8>(s0); s0 = xrow_sum(s0);
    s1 += dpp<ROR8>(s1); s1 = xrow_sum(s1);
    if (lane < 8) *reinterpret_cast<f32x2*>(fin_s + wave * 16 + lane * 2) = f32x2{s0, s1};
    __syncthreads();
    if (wave == 0 && lane < 8) {
      float sa = 0.f, sb = 0.f;
#pragma unroll
      for (int v = 0; v < 8; ++v) { const f32x2 t = *reinterpret_cast<const f32x2*>(fin_s + v * 16 + lane * 2); sa += t[0]; sb += t[1]; }
      const f32x2 g = *reinterpret_cast<const f32x2*>(g_s + lane * 2);
      const float r0 = bf_lo(xr) + rbf(rbf(sa) * g[0]), r1 = bf_hi(xr) + rbf(rbf(sb) * g[1]);
      *reinterpret_cast<unsigned*>(p.out + oidx) = cvtpk(r0, r1);
      if (w == 0 && lane == 0) __hip_atomic_store(reinterpret_cast<u64*>(p.ws + WS_EPOCH), tag, __ATOMIC_RELAXED, __HIP_MEMORY_SCOPE_AGENT);
    }
  }
  STAMP(7);
}

}

extern "C" void kernel_launch(void* const* d_in, const int* in_sizes, int n_in, void* d_out, int out_size, void* d_ws, size_t ws_size,
                              hipStream_t stream) {
  Params p;
  p.x = (const unsigned short*)d_in[0];
  p.k = (const unsigned short*)d_in[1];
  p.v = (const unsigned short*)d_in[2];
  p.wq = (const unsigned short*)d_in[3];
  p.wo = (const unsigned short*)d_in[4];
  p.w1 = (const unsigned short*)d_in[6];
  p.w2 = (const unsigned short*)d_in[7];
  p.out = (unsigned short*)d_out;
  p.ws = (unsigned char*)d_ws;
  hipLaunchKernelGGL(fused_decode_kernel, dim3(NWG), dim3(NTHR), 0, stream, p);
}
```

```cpp
#include <hip/hip_runtime.h>
#include <hip/hip_bf16.h>
#include <stdint.h>

typedef unsigned u32x4 __attribute__((ext_vector_type(4)));
typedef unsigned u32x2 __attribute__((ext_vector_type(2)));
typedef float f32x4 __attribute__((ext_vector_type(4)));
typedef float f32x2 __attribute__((ext_vector_type(2)));
typedef __bf16 bf16x2 __attribute__((ext_vector_type(2)));
typedef __bf16 bf16x8 __attribute__((ext_vector_type(8)));
typedef short s16x4 __attribute__((ext_vector_type(4)));
typedef unsigned long long u64;

namespace {

constexpr int NWG = 256, NTHR = 512, NWAVE = 8;
constexpr int SK = 32768, DH = 128, DM = 512, DFF = 1792, NQ = 4096;
constexpr int DEPTH = 6;
constexpr int NFULL = 4, REM = 2;
static_assert(4 * (NFULL * DEPTH + REM) + 4 * DEPTH == 128, "128 tiles of 16 keys per workgroup");
constexpr int NFF = DFF / 8;
constexpr unsigned WS_EPOCH = 0;
constexpr unsigned WS_QG = 4096;
constexpr unsigned WS_HP = WS_QG + 4 * 2 * NQ * 8;
constexpr unsigned WS_AP = WS_HP + 8 * 28 * 128 * 8;
constexpr unsigned WS_ST = WS_AP + NWG * 4 * 128 * 8;
constexpr unsigned WS_YP = WS_ST + 8 * 4 * 32 * 4 * 8;
constexpr unsigned WS_SP = WS_YP + 28 * 1024 * 8;
constexpr unsigned WS_END = WS_SP + 64 * 1024 * 8;
constexpr unsigned WS_DBG = 8u << 20;
constexpr unsigned SPIN_CAP = 1u << 16;
constexpr int AUX_NT = 2, AUX_SC1 = 16;
constexpr float SOFT_C = 0.08838834764831845f * 1.4426950408889634f;

__device__ __forceinline__ float bf_lo(unsigned u) { return __uint_as_float(u << 16); }
__device__ __forceinline__ float bf_hi(unsigned u) { return __uint_as_float(u & 0xffff0000u); }
__device__ __forceinline__ unsigned cvtpk(float lo, float hi) {
  return __builtin_bit_cast(unsigned, __builtin_convertvector(f32x2{lo, hi}, bf16x2));
}
__device__ __forceinline__ float rbf(float x) { return bf_lo(cvtpk(x, x)); }
template <int CTRL> __device__ __forceinline__ float dpp(float x) {
  return __builtin_bit_cast(float, __builtin_amdgcn_mov_dpp(__builtin_bit_cast(int, x), CTRL, 0xf, 0xf, true));
}
constexpr int ROR1 = 0x121, ROR2 = 0x122, ROR4 = 0x124, ROR8 = 0x128, XOR1 = 0xB1, XOR2 = 0x4E, XOR7 = 0x141;
__device__ __forceinline__ float xrow_max(float x) {
  auto s = __builtin_amdgcn_permlane16_swap(__float_as_uint(x), __float_as_uint(x), false, false);
  x = fmaxf(__uint_as_float(s[0]), __uint_as_float(s[1]));
  auto t = __builtin_amdgcn_permlane32_swap(__float_as_uint(x), __float_as_uint(x), false, false);
  return fmaxf(__uint_as_float(t[0]), __uint_as_float(t[1]));
}
__device__ __forceinline__ float xrow_sum(float x) {
  auto s = __builtin_amdgcn_permlane16_swap(__float_as_uint(x), __float_as_uint(x), false, false);
  x = __uint_as_float(s[0]) + __uint_as_float(s[1]);
  auto t = __builtin_amdgcn_permlane32_swap(__float_as_uint(x), __float_as_uint(x), false, false);
  return __uint_as_float(t[0]) + __uint_as_float(t[1]);
}
__device__ __forceinline__ float wave_sum(float x) {
  x += dpp<ROR8>(x); x += dpp<ROR4>(x); x += dpp<ROR2>(x); x += dpp<ROR1>(x);
  return xrow_sum(x);
}
__device__ __forceinline__ float tr32(float a, float b) {
  auto t = __builtin_amdgcn_permlane32_swap(__float_as_uint(a), __float_as_uint(b), false, false);
  return __uint_as_float(t[0]) + __uint_as_float(t[1]);
}
__device__ __forceinline__ float tr16(float a, float b) {
  auto t = __builtin_amdgcn_permlane16_swap(__float_as_uint(a), __float_as_uint(b), false, false);
  return __uint_as_float(t[0]) + __uint_as_float(t[1]);
}
__device__ __forceinline__ float tr8(float a, float b, bool bit3) {
  const float keep = bit3 ? b : a, send = bit3 ? a : b;
  return keep + dpp<ROR8>(send);
}
__device__ __forceinline__ float tr4(float a, float b, bool bit2) {
  const float keep = bit2 ? b : a, send = bit2 ? a : b;
  return keep + dpp<XOR7>(send);
}
__device__ __forceinline__ unsigned f24(float x) { return (__float_as_uint(x) + 0x80u) >> 8; }
__device__ __forceinline__ u32x2 gr24(unsigned payload24, unsigned tlo8, unsigned thi) { return u32x2{payload24 | tlo8, thi}; }
__device__ __forceinline__ bool ok24(unsigned lo, unsigned hi, unsigned tlo8, unsigned thi) { return ((lo & 0xff000000u) == tlo8) & (hi == thi); }
__device__ __forceinline__ float val24(unsigned lo) { return __uint_as_float(lo << 8); }

struct Params {
  const unsigned short* x; const unsigned short* k; const unsigned short* v; const unsigned short* wq; const unsigned short* wo;
  const unsigned short* w1; const unsigned short* w2; unsigned short* out; unsigned char* ws;
};

#define START_WIN 2
#define WARM_LIVE0 0
#define STAMP(i) do {} while (0)
#define LDW1(p_) LDNT(p_)
#define Q_DELAY 32
#define EARLY_GO 3
#define YP_DELAY 1
#define AUX_K 2
#define AUX_V 2
#define PRE_BAR 2
#define LDNT(p_) __builtin_nontemporal_load(reinterpret_cast<const u32x4*>(p_))

__device__ __forceinline__ void layernorm_to_lds(const u32x4 xa, const u32x4 xb, float* dst, int lane) {
  float e0[8], e1[8];
#pragma unroll
  for (int j = 0; j < 4; ++j) { e0[2 * j] = bf_lo(xa[j]); e0[2 * j + 1] = bf_hi(xa[j]); e1[2 * j] = bf_lo(xb[j]); e1[2 * j + 1] = bf_hi(xb[j]); }
  float s0 = 0.f, s1 = 0.f;
#pragma unroll
  for (int j = 0; j < 8; ++j) { s0 += e0[j]; s1 += e1[j]; }
  s0 = wave_sum(s0) * (1.f / DM); s1 = wave_sum(s1) * (1.f / DM);
  float v0 = 0.f, v1 = 0.f;
#pragma unroll
  for (int j = 0; j < 8; ++j) { const float a = e0[j] - s0, b = e1[j] - s1; v0 = fmaf(a, a, v0); v1 = fmaf(b, b, v1); }
  v0 = wave_sum(v0) * (1.f / DM); v1 = wave_sum(v1) * (1.f / DM);
  const float m0 = rbf(s0), m1 = rbf(s1);
  const float d0 = 1.f / rbf(sqrtf(rbf(rbf(v0) + 1e-5f))), d1 = 1.f / rbf(sqrtf(rbf(rbf(v1) + 1e-5f)));
#pragma unroll
  for (int j = 0; j < 8; j += 2)
    *reinterpret_cast<f32x4*>(dst + (8 * lane + j) * 2) =
        f32x4{rbf(rbf(e0[j] - m0) * d0), rbf(rbf(e1[j] - m1) * d1), rbf(rbf(e0[j + 1] - m0) * d0), rbf(rbf(e1[j + 1] - m1) * d1)};
}

__device__ __forceinline__ bool ok24x2(const u32x4 g, unsigned tlo8, unsigned thi) { return ok24(g[0], g[1], tlo8, thi) & ok24(g[2], g[3], tlo8, thi); }
__device__ __forceinline__ u32x4 gr24x2(float a, float b, unsigned tlo8, unsigned thi) { return u32x4{f24(a) | tlo8, thi, f24(b) | tlo8, thi}; }

__global__ void __launch_bounds__(NTHR, 2) fused_decode_kernel(Params p) {
  __shared__ __attribute__((aligned(16))) float lds[13312];
  float* xn_s = lds;
  float* red_q = lds + 1024;
  float* mrg_m = lds + 1536;
  float* mrg_l = lds + 1568;
  float* c_scl = lds + 1600;
  float* fin_s = lds + 1632;
  float* tr_h = lds + 1760;
  float* h_s = lds + 1888;
  float* g_s = lds + 2016;
  unsigned* q_s = reinterpret_cast<unsigned*>(lds + 2048);
  unsigned* flag_w = reinterpret_cast<unsigned*>(lds + 2304);
  float* c_acc = lds + 2560;
  float* mrg_o = lds + 4608;
  u32x4* wo_s = reinterpret_cast<u32x4*>(lds + 8704);

  const int w = blockIdx.x, tid = threadIdx.x, lane = tid & 63;
  const int wave = __builtin_amdgcn_readfirstlane(tid >> 6);
  const int kvh = w & 7, bat = (w >> 3) & 1, split = w >> 4, idx = w >> 3;
  const int n16 = lane & 15, g4 = lane >> 4, frow = lane >> 3, fcc = lane & 7;
  STAMP(0);

  const __amdgpu_buffer_rsrc_t wsr = __builtin_amdgcn_make_buffer_rsrc((void*)p.ws, 0, WS_END, 0x00020000);
#define LOAD_EPOCH() __hip_atomic_load(reinterpret_cast<const u64*>(p.ws + WS_EPOCH), __ATOMIC_RELAXED, __HIP_MEMORY_SCOPE_AGENT)
#define WG_BARRIER() do { __builtin_amdgcn_sched_barrier(0); asm volatile("s_waitcnt lgkmcnt(0)\n\ts_barrier" ::: "memory"); __builtin_amdgcn_sched_barrier(0); } while (0)
#define LDG(off_) __builtin_amdgcn_raw_buffer_load_b128(wsr, (off_), 0, AUX_SC1)
#define STG(v_, off_) __builtin_amdgcn_raw_buffer_store_b128((v_), wsr, (off_), 0, AUX_SC1)
  const bool streamer = wave >= 4;

#define SET_FLAG(i_) __hip_atomic_store(&flag_w[i_], 1u, __ATOMIC_RELEASE, __HIP_MEMORY_SCOPE_WORKGROUP)
#define GET_FLAG(i_) (__hip_atomic_load(&flag_w[i_], __ATOMIC_ACQUIRE, __HIP_MEMORY_SCOPE_WORKGROUP) != 0u)
  const __amdgpu_buffer_rsrc_t kr = __builtin_amdgcn_make_buffer_rsrc((void*)p.k, 0, 2u * 8 * SK * DH * 2, 0x00020000);
  const __amdgpu_buffer_rsrc_t vr = __builtin_amdgcn_make_buffer_rsrc((void*)p.v, 0, 2u * 8 * SK * DH * 2, 0x00020000);
  const unsigned kvoff = (unsigned)(((n16 & 3) * 4 + (n16 >> 2)) * 256 + g4 * 16);
  const unsigned vvoff = (unsigned)lane * 16;
  const unsigned sbase = (unsigned)((bat * 8 + kvh) * SK + split * 2048 + ((streamer ? 4 * DEPTH : 0) + (wave & 3)) * 16) * (DH * 2);
  constexpr unsigned TSTEP = 4 * 16 * DH * 2;
  u32x4 kb[DEPTH][4], vb[DEPTH][4];
#define ISSUE(d_, so_) do { \
    _Pragma("unroll") for (int s_ = 0; s_ < 4; ++s_) kb[d_][s_] = __builtin_amdgcn_raw_buffer_load_b128(kr, kvoff + s_ * 64, (so_), AUX_K); \
    _Pragma("unroll") for (int s_ = 0; s_ < 4; ++s_) vb[d_][s_] = __builtin_amdgcn_raw_buffer_load_b128(vr, vvoff + s_ * 1024, (so_), AUX_V); \
    __builtin_amdgcn_sched_barrier(0); } while (0)
  u64 tag;
  if (__builtin_expect(streamer, 1)) {
    STAMP(9);
#pragma unroll
    for (int d = 0; d < PRE_BAR; ++d) ISSUE(d, sbase + d * TSTEP);
    WG_BARRIER();
#pragma unroll
    for (int d = PRE_BAR; d < DEPTH; ++d) ISSUE(d, sbase + d * TSTEP);
    tag = LOAD_EPOCH() + 1;
  } else {
    const u32x4 xa = *reinterpret_cast<const u32x4*>(p.x + 8 * lane);
    const u32x4 xb = *reinterpret_cast<const u32x4*>(p.x + DM + 8 * lane);
    tag = LOAD_EPOCH() + 1;
    __builtin_amdgcn_sched_barrier(0);
    const int qrow = (w >> 6) * 128 + wave * 32 + frow;
    const unsigned short* wqp = p.wq + (size_t)qrow * NQ + (w & 63) * 64 + fcc * 8;
    u32x4 wv[4];
#pragma unroll
    for (int k = 0; k < 4; ++k) wv[k] = LDNT(wqp + (size_t)k * 8 * NQ);
    if (tid < 16) flag_w[tid] = 0u;
    WG_BARRIER();
    layernorm_to_lds(xa, xb, xn_s, lane);
    const unsigned t40_lo = (unsigned)(tag << 24), t40_hi = (unsigned)(tag >> 8);
    const bool b3 = (lane >> 3) & 1;
    {
      float qa[16];
#pragma unroll
      for (int j = 0; j < 16; ++j) qa[j] = 0.f;
#pragma unroll
      for (int k = 0; k < 4; ++k) {
        const f32x2 xv = *reinterpret_cast<const f32x2*>(xn_s + (qrow + 8 * k) * 2);
#pragma unroll
        for (int j = 0; j < 4; ++j) {
          const float l0 = bf_lo(wv[k][j]), h0 = bf_hi(wv[k][j]);
          qa[2 * j] = fmaf(xv[0], l0, qa[2 * j]); qa[2 * j + 1] = fmaf(xv[0], h0, qa[2 * j + 1]);
          qa[8 + 2 * j] = fmaf(xv[1], l0, qa[8 + 2 * j]); qa[8 + 2 * j + 1] = fmaf(xv[1], h0, qa[8 + 2 * j + 1]);
        }
      }
#pragma unroll
      for (int j = 0; j < 8; ++j) qa[j] = tr32(qa[j], qa[j + 8]);
#pragma unroll
      for (int j = 0; j < 4; ++j) qa[j] = tr16(qa[j], qa[j + 4]);
      *reinterpret_cast<f32x2*>(red_q + wave * 128 + (lane >> 5) * 64 + fcc * 8 + ((lane >> 3) & 3) * 2) = f32x2{tr8(qa[0], qa[2], b3), tr8(qa[1], qa[3], b3)};
      SET_FLAG(wave);
    }
    u32x4 w1v[8];
    const bool has_f1 = w < NFF;
    const int f1row = (w / 28) * 64 + frow;
    if (wave == 0) {
      while (!__all(GET_FLAG(lane & 3))) __builtin_amdgcn_s_sleep(0);
      f32x2 t = *reinterpret_cast<const f32x2*>(red_q + 2 * lane);
#pragma unroll
      for (int v = 1; v < 4; ++v) t += *reinterpret_cast<const f32x2*>(red_q + v * 128 + 2 * lane);
      STG(gr24x2(t[0], t[1], t40_lo, t40_hi), WS_QG + (unsigned)(((w >> 6) * 2 + (lane >> 5)) * NQ + (w & 63) * 64 + (lane & 31) * 2) * 8);
      STAMP(1);
    } else if (wave == 1 && has_f1) {
#pragma unroll
      for (int k = 0; k < 8; ++k) w1v[k] = LDNT(p.w1 + (size_t)(f1row + 8 * k) * DFF + (w % 28) * 64 + fcc * 8);
    }
    __builtin_amdgcn_sched_barrier(0);
    {
      const unsigned qoff = WS_QG + (unsigned)(bat * NQ + kvh * 512 + wave * 128 + 2 * lane) * 8;
      u32x4 qg[4];
      unsigned spins = 0;
      __builtin_amdgcn_s_sleep(Q_DELAY);
#pragma unroll
      for (int k = 0; k < 4; ++k) qg[k] = LDG(qoff + k * (2 * NQ * 8));
      for (;;) {
        const bool k0 = __all(ok24x2(qg[0], t40_lo, t40_hi)), k1 = __all(ok24x2(qg[1], t40_lo, t40_hi));
        const bool k2 = __all(ok24x2(qg[2], t40_lo, t40_hi)), k3 = __all(ok24x2(qg[3], t40_lo, t40_hi));
        if ((k0 & k1 & k2 & k3) || ++spins >= SPIN_CAP) break;
        __builtin_amdgcn_s_sleep(8);
        asm volatile("" ::: "memory");
        if (!k0) qg[0] = LDG(qoff);
        if (!k1) qg[1] = LDG(qoff + 1 * (2 * NQ * 8));
        if (!k2) qg[2] = LDG(qoff + 2 * (2 * NQ * 8));
        if (!k3) qg[3] = LDG(qoff + 3 * (2 * NQ * 8));
      }
      q_s[wave * 64 + lane] = cvtpk((val24(qg[0][0]) + val24(qg[1][0])) + (val24(qg[2][0]) + val24(qg[3][0])),
                                    (val24(qg[0][2]) + val24(qg[1][2])) + (val24(qg[2][2]) + val24(qg[3][2])));
      SET_FLAG(4 + wave);
      STAMP(11);
    }
    if (wave == 1 && has_f1) {
      float ha[16];
#pragma unroll
      for (int j = 0; j < 16; ++j) ha[j] = 0.f;
#pragma unroll
      for (int k = 0; k < 8; ++k) {
        const f32x2 xv = *reinterpret_cast<const f32x2*>(xn_s + (f1row + 8 * k) * 2);
#pragma unroll
        for (int j = 0; j < 4; ++j) {
          const float l0 = bf_lo(w1v[k][j]), h0 = bf_hi(w1v[k][j]);
          ha[2 * j] = fmaf(xv[0], l0, ha[2 * j]); ha[2 * j + 1] = fmaf(xv[0], h0, ha[2 * j + 1]);
          ha[8 + 2 * j] = fmaf(xv[1], l0, ha[8 + 2 * j]); ha[8 + 2 * j + 1] = fmaf(xv[1], h0, ha[8 + 2 * j + 1]);
        }
      }
#pragma unroll
      for (int j = 0; j < 8; ++j) ha[j] = tr32(ha[j], ha[j + 8]);
#pragma unroll
      for (int j = 0; j < 4; ++j) ha[j] = tr16(ha[j], ha[j + 4]);
      { const int u = fcc * 8 + (frow & 3) * 2;
        tr_h[u * 2 + (lane >> 5)] = tr8(ha[0], ha[2], b3); tr_h[u * 2 + 2 + (lane >> 5)] = tr8(ha[1], ha[3], b3); }
      const f32x2 t = *reinterpret_cast<const f32x2*>(tr_h + 2 * lane);
      STG(gr24x2(t[0], t[1], t40_lo, t40_hi), WS_HP + (unsigned)(w * 128 + 2 * lane) * 8);
      STAMP(10);
    } else if (wave == 2) {
      u32x4 t[16];
#pragma unroll
      for (int k = 0; k < 16; ++k) t[k] = LDNT(p.wo + (size_t)(kvh * 512 + (idx >> 2) * 64 + k * 4 + g4) * DM + (idx & 3) * 128 + n16 * 8);
#pragma unroll
      for (int k = 0; k < 16; ++k) wo_s[(k * 4 + g4) * 17 + n16] = t[k];
    }
    __builtin_amdgcn_sched_barrier(0);
#pragma unroll
    for (int d = 0; d < DEPTH; ++d) ISSUE(d, sbase + d * TSTEP);
  }

  {
    const unsigned msk0 = (n16 & 3) == 0 ? 0x0000ffffu : (n16 & 3) == 1 ? 0xffff0000u : 0u;
    const unsigned msk1 = (n16 & 3) == 2 ? 0x0000ffffu : (n16 & 3) == 3 ? 0xffff0000u : 0u;
    float m_run = -INFINITY, l_run = 0.f;
    f32x4 olo = {0.f, 0.f, 0.f, 0.f}, ohi = {0.f, 0.f, 0.f, 0.f};
    while (!__all(GET_FLAG(4 + (lane & 3)))) __builtin_amdgcn_s_sleep(1);
    bf16x8 qf[4];
#pragma unroll
    for (int s = 0; s < 4; ++s) qf[s] = *reinterpret_cast<const bf16x8*>(q_s + ((n16 >> 2) * DH + s * 32 + g4 * 8) / 2);
    STAMP(2);
#define TILE(d) do { \
      f32x4 sc = {0.f, 0.f, 0.f, 0.f}; \
      _Pragma("unroll") for (int s = 0; s < 4; ++s) sc = __builtin_amdgcn_mfma_f32_16x16x32_bf16(__builtin_bit_cast(bf16x8, kb[d][s]), qf[s], sc, 0, 0, 0); \
      float tmax = fmaxf(fmaxf(sc[0], sc[1]), fmaxf(sc[2], sc[3])); \
      tmax = xrow_max(tmax); \
      const float mnew = fmaxf(m_run, tmax); \
      const float alpha = __builtin_amdgcn_exp2f((m_run - mnew) * SOFT_C); \
      const float mc = -mnew * SOFT_C; \
      float pr[4]; \
      _Pragma("unroll") for (int r = 0; r < 4; ++r) pr[r] = __builtin_amdgcn_exp2f(fmaf(sc[r], SOFT_C, mc)); \
      l_run = fmaf(l_run, alpha, (pr[0] + pr[1]) + (pr[2] + pr[3])); \
      m_run = mnew; \
      olo *= alpha; ohi *= alpha; \
      _Pragma("unroll") for (int r = 0; r < 4; ++r) { \
        const unsigned pk = cvtpk(pr[r], pr[r]); \
        const u32x2 bp = {pk & msk0, pk & msk1}; \
        const u32x2 vlo = {vb[d][r][0], vb[d][r][1]}, vhi = {vb[d][r][2], vb[d][r][3]}; \
        olo = __builtin_amdgcn_mfma_f32_16x16x16bf16_1k(__builtin_bit_cast(s16x4, vlo), __builtin_bit_cast(s16x4, bp), olo, 0, 0, 0); \
        ohi = __builtin_amdgcn_mfma_f32_16x16x16bf16_1k(__builtin_bit_cast(s16x4, vhi), __builtin_bit_cast(s16x4, bp), ohi, 0, 0, 0); \
      } \
      __builtin_amdgcn_sched_barrier(0); } while (0)
    if (streamer) {
#pragma unroll
      for (int d = 0; d < REM; ++d) { TILE(d); ISSUE(d, sbase + (unsigned)(DEPTH + d) * TSTEP); }
#pragma unroll 1
      for (int it = 0; it < NFULL - 1; ++it) {
        const unsigned snext = sbase + (unsigned)(REM + (it + 1) * DEPTH) * TSTEP;
#pragma unroll
        for (int d = 0; d < DEPTH; ++d) {
          TILE((d + REM) % DEPTH);
          ISSUE((d + REM) % DEPTH, snext + d * TSTEP);
        }
      }
#pragma unroll
      for (int d = 0; d < DEPTH; ++d) TILE((d + REM) % DEPTH);
    } else {
#pragma unroll
      for (int d = 0; d < DEPTH; ++d) TILE(d);
    }
#undef TILE
#undef ISSUE
    STAMP(3);
    l_run = xrow_sum(l_run);
    float* mo = mrg_o + wave * 512 + (n16 >> 2) * DH + g4 * 32 + (n16 & 3);
#pragma unroll
    for (int r = 0; r < 4; ++r) { mo[r * 8] = olo[r]; mo[r * 8 + 4] = ohi[r]; }
    if (g4 == 0 && (n16 & 3) == 0) { mrg_m[wave * 4 + (n16 >> 2)] = m_run; mrg_l[wave * 4 + (n16 >> 2)] = l_run; }
  }
  const unsigned t40_lo = (unsigned)(tag << 24), t40_hi = (unsigned)(tag >> 8);
  if (wave == 3) {
    const bool b3 = (lane >> 3) & 1;
    if (w < NFF) {
      u32x4 w2v[8];
#pragma unroll
      for (int k = 0; k < 8; ++k) w2v[k] = LDNT(p.w2 + (size_t)((w >> 3) * 64 + frow + 8 * k) * DM + (w & 7) * 64 + fcc * 8);
      const unsigned hoff = WS_HP + (unsigned)((w >> 3) * 128 + 2 * lane) * 8;
      u32x4 hg[8];
      unsigned spins = 0;
      for (;;) {
        bool ok = true;
#pragma unroll
        for (int k = 0; k < 8; ++k) hg[k] = LDG(hoff + k * (28 * 128 * 8));
#pragma unroll
        for (int k = 0; k < 8; ++k) ok &= ok24x2(hg[k], t40_lo, t40_hi);
        if (__all(ok) || ++spins >= SPIN_CAP) break;
        __builtin_amdgcn_s_sleep(32);
        asm volatile("" ::: "memory");
      }
      float h0 = 0.f, h1 = 0.f;
#pragma unroll
      for (int k = 0; k < 8; ++k) { h0 += val24(hg[k][0]); h1 += val24(hg[k][2]); }
      *reinterpret_cast<f32x2*>(h_s + 2 * lane) = f32x2{fmaxf(rbf(h0), 0.f), fmaxf(rbf(h1), 0.f)};
      float ya[16];
#pragma unroll
      for (int j = 0; j < 16; ++j) ya[j] = 0.f;
#pragma unroll
      for (int k = 0; k < 8; ++k) {
        const f32x2 hv = *reinterpret_cast<const f32x2*>(h_s + (frow + 8 * k) * 2);
#pragma unroll
        for (int j = 0; j < 4; ++j) {
          const float l0 = bf_lo(w2v[k][j]), hh = bf_hi(w2v[k][j]);
          ya[2 * j] = fmaf(hv[0], l0, ya[2 * j]); ya[2 * j + 1] = fmaf(hv[0], hh, ya[2 * j + 1]);
          ya[8 + 2 * j] = fmaf(hv[1], l0, ya[8 + 2 * j]); ya[8 + 2 * j + 1] = fmaf(hv[1], hh, ya[8 + 2 * j + 1]);
        }
      }
#pragma unroll
      for (int j = 0; j < 8; ++j) ya[j] = tr32(ya[j], ya[j + 8]);
#pragma unroll
      for (int j = 0; j < 4; ++j) ya[j] = tr16(ya[j], ya[j + 4]);
      asm volatile("" ::: "memory");
      *reinterpret_cast<f32x2*>(h_s + ((fcc >> 1) * 2 + (lane >> 5)) * 16 + ((frow & 3) * 2 + (fcc & 1)) * 2) = f32x2{tr8(ya[0], ya[2], b3), tr8(ya[1], ya[3], b3)};
      const f32x2 t = *reinterpret_cast<const f32x2*>(h_s + 2 * lane);
      STG(gr24x2(t[0], t[1], t40_lo, t40_hi), WS_YP + (unsigned)((w >> 3) * 1024 + (w & 7) * 128 + 2 * lane) * 8);
    }
    STAMP(8);
    if (w < 64) {
      const unsigned yoff = WS_YP + (unsigned)(frow * 1024 + w * 16 + fcc * 2) * 8;
      u32x4 yg[4];
      unsigned spins = 0;
      for (;;) {
        __builtin_amdgcn_s_sleep(64);
        bool ok = true;
#pragma unroll
        for (int k = 0; k < 4; ++k) yg[k] = (k < 3 || lane < 32) ? LDG(yoff + k * 8 * 8192) : u32x4{t40_lo, t40_hi, t40_lo, t40_hi};
#pragma unroll
        for (int k = 0; k < 4; ++k) ok &= ok24x2(yg[k], t40_lo, t40_hi);
        if (__all(ok) || ++spins >= SPIN_CAP) break;
        asm volatile("" ::: "memory");
      }
      float y0 = (val24(yg[0][0]) + val24(yg[1][0])) + (val24(yg[2][0]) + val24(yg[3][0]));
      float y1 = (val24(yg[0][2]) + val24(yg[1][2])) + (val24(yg[2][2]) + val24(yg[3][2]));
      y0 += dpp<ROR8>(y0); y0 = rbf(xrow_sum(y0));
      y1 += dpp<ROR8>(y1); y1 = rbf(xrow_sum(y1));
      if (lane < 8) *reinterpret_cast<f32x2*>(g_s + lane * 2) = f32x2{rbf(y0 / (1.f + __expf(-y0))), rbf(y1 / (1.f + __expf(-y1)))};
    }
    STAMP(12);
  }
  asm volatile("" :: "v"(t40_lo), "v"(t40_hi));
  __syncthreads();
  {
    const int h = tid >> 7;
    float mw[8], M = -INFINITY;
#pragma unroll
    for (int v = 0; v < 8; ++v) { mw[v] = mrg_m[v * 4 + h]; M = fmaxf(M, mw[v]); }
    float o = 0.f, L = 0.f;
#pragma unroll
    for (int v = 0; v < 8; ++v) {
      const float f = __builtin_amdgcn_exp2f((mw[v] - M) * SOFT_C);
      o = fmaf(f, mrg_o[v * 512 + tid], o); L = fmaf(f, mrg_l[v * 4 + h], L);
    }
    __builtin_amdgcn_raw_buffer_store_b64(gr24(f24(o), t40_lo, t40_hi), wsr, WS_AP + (unsigned)(w * 512 + tid) * 8, 0, AUX_SC1);
    if ((tid & 127) < 2) {
      const unsigned mb = __float_as_uint(M);
      const unsigned p0 = (tid & 1) ? f24(L) : (mb >> 8), p1 = (tid & 1) ? 0u : (mb & 0xffu);
      STG((u32x4{p0 | t40_lo, t40_hi, p1 | t40_lo, t40_hi}), WS_ST + (unsigned)(((kvh * 4 + h) * 32 + bat * 16 + split) * 4 + 2 * (tid & 1)) * 8);
    }
  }
  STAMP(4);
  float wof[16];
  {
    const u32x4 woa = wo_s[(lane >> 1) * 17 + wave * 2 + (lane & 1)], wob = wo_s[(32 + (lane >> 1)) * 17 + wave * 2 + (lane & 1)];
#pragma unroll
    for (int j = 0; j < 4; ++j) { wof[2 * j] = bf_lo(woa[j]); wof[2 * j + 1] = bf_hi(woa[j]); wof[8 + 2 * j] = bf_lo(wob[j]); wof[8 + 2 * j + 1] = bf_hi(wob[j]); }
  }
  {
    if (wave == 0) {
      const unsigned o2 = WS_ST + (unsigned)((kvh * 4 + (idx >> 3)) * 128 + lane * 2) * 8;
      u32x4 gs;
      unsigned spins = 0;
      for (;;) {
        gs = LDG(o2);
        const int missing = __popcll(__ballot(!ok24x2(gs, t40_lo, t40_hi)));
        if (missing <= 2 * EARLY_GO) SET_FLAG(8);
        if (missing == 0 || ++spins >= SPIN_CAP) break;
        asm volatile("" ::: "memory");
      }
      SET_FLAG(8);
      const float mine = __uint_as_float((gs[0] << 8) | ((lane & 1) ? 0u : (gs[2] & 0xffu)));
      const float other = dpp<XOR1>(mine);
      const float m = (lane & 1) ? other : mine, l = (lane & 1) ? mine : other;
      float M = m; M = fmaxf(M, dpp<XOR2>(M)); M = fmaxf(M, dpp<XOR7>(M)); M = fmaxf(M, dpp<ROR8>(M));
      { auto t = __builtin_amdgcn_permlane16_swap(__float_as_uint(M), __float_as_uint(M), false, false); M = fmaxf(__uint_as_float(t[0]), __uint_as_float(t[1])); }
      const float f = __builtin_amdgcn_exp2f((m - M) * SOFT_C);
      float L = f * l; L += dpp<XOR2>(L); L += dpp<XOR7>(L); L += dpp<ROR8>(L); L = tr16(L, L);
      if ((lane & 1) == 0) c_scl[lane >> 1] = f / L;
    } else {
      while (!GET_FLAG(8)) __builtin_amdgcn_s_sleep(0);
    }
    const int pp0 = wave * 4 + (lane >> 5), pp1 = pp0 + 2;
    const unsigned o1 = WS_AP + (unsigned)((kvh + 8 * (pp0 >> 4) + 16 * (pp0 & 15)) * 512 + (idx >> 2) * 64 + (lane & 31) * 2) * 8;
    u32x4 ga, gb;
    unsigned spins = 0;
    for (;;) {
      ga = LDG(o1); gb = LDG(o1 + 2 * 16 * 512 * 8);
      if (__all(ok24x2(ga, t40_lo, t40_hi) & ok24x2(gb, t40_lo, t40_hi)) || ++spins >= SPIN_CAP) break;
      asm volatile("" ::: "memory");
    }
    *reinterpret_cast<f32x2*>(c_acc + pp0 * 64 + (lane & 31) * 2) = f32x2{val24(ga[0]), val24(ga[2])};
    *reinterpret_cast<f32x2*>(c_acc + pp1 * 64 + (lane & 31) * 2) = f32x2{val24(gb[0]), val24(gb[2])};
  }
  __syncthreads();
  STAMP(5);
  {
    float o0 = 0.f, o1 = 0.f;
#pragma unroll
    for (int s4 = 0; s4 < 4; ++s4) {
      const f32x4 sa = *reinterpret_cast<const f32x4*>(c_scl + s4 * 4), sb = *reinterpret_cast<const f32x4*>(c_scl + 16 + s4 * 4);
#pragma unroll
      for (int j = 0; j < 4; ++j) { o0 = fmaf(sa[j], c_acc[(s4 * 4 + j) * 64 + lane], o0); o1 = fmaf(sb[j], c_acc[(16 + s4 * 4 + j) * 64 + lane], o1); }
    }
    o0 = rbf(o0); o1 = rbf(o1);
    const float o00 = __shfl(o0, lane >> 1), o01 = __shfl(o0, 32 + (lane >> 1)), o10 = __shfl(o1, lane >> 1), o11 = __shfl(o1, 32 + (lane >> 1));
    float sa[8];
#pragma unroll
    for (int j = 0; j < 8; ++j) sa[j] = tr32(fmaf(o01, wof[8 + j], o00 * wof[j]), fmaf(o11, wof[8 + j], o10 * wof[j]));
#pragma unroll
    for (int j = 0; j < 4; ++j) sa[j] = tr16(sa[j], sa[j + 4]);
    const bool b1 = (lane >> 1) & 1;
    float r0 = (b1 ? sa[2] : sa[0]) + dpp<XOR2>(b1 ? sa[0] : sa[2]), r1 = (b1 ? sa[3] : sa[1]) + dpp<XOR2>(b1 ? sa[1] : sa[3]);
    r0 += dpp<ROR4>(r0); r0 += dpp<ROR8>(r0);
    r1 += dpp<ROR4>(r1); r1 += dpp<ROR8>(r1);
    if ((lane & 12) == 0)
      STG(gr24x2(r0, r1, t40_lo, t40_hi),
          WS_SP + (unsigned)((kvh * 8 + (idx >> 2)) * 1024 + (((idx & 3) * 8 + wave) * 2 + (lane >> 5)) * 16 + (((lane >> 4) & 1) * 4 + (lane & 3)) * 2) * 8);
  }
  STAMP(6);
  if (w < 64) {
    const unsigned soff = WS_SP + (unsigned)((wave * 8 + frow) * 1024 + w * 16 + fcc * 2) * 8;
    const int oidx = (w & 1) * DM + (w >> 1) * 16 + (fcc & 1) * 8 + (fcc >> 2) * 4 + ((fcc >> 1) & 1) * 2;
    const unsigned xr = *reinterpret_cast<const unsigned*>(p.x + oidx);
    u32x4 sg;
    unsigned spins = 0;
    for (;;) {
      sg = LDG(soff);
      if (__all(ok24x2(sg, t40_lo, t40_hi)) || ++spins >= SPIN_CAP) break;
      asm volatile("" ::: "memory");
    }
    float s0 = val24(sg[0]), s1 = val24(sg[2]);
    s0 += dpp<ROR8>(s0); s0 = xrow_sum(s0);
    s1 += dpp<ROR8>(s1); s1 = xrow_sum(s1);
    if (lane < 8) *reinterpret_cast<f32x2*>(fin_s + wave * 16 + lane * 2) = f32x2{s0, s1};
    __syncthreads();
    if (wave == 0 && lane < 8) {
      float sa = 0.f, sb = 0.f;
#pragma unroll
      for (int v = 0; v < 8; ++v) { const f32x2 t = *reinterpret_cast<const f32x2*>(fin_s + v * 16 + lane * 2); sa += t[0]; sb += t[1]; }
      const f32x2 g = *reinterpret_cast<const f32x2*>(g_s + lane * 2);
      const float r0 = bf_lo(xr) + rbf(rbf(sa) * g[0]), r1 = bf_hi(xr) + rbf(rbf(sb) * g[1]);
      *reinterpret_cast<unsigned*>(p.out + oidx) = cvtpk(r0, r1);
      if (w == 0 && lane == 0) __hip_atomic_store(reinterpret_cast<u64*>(p.ws + WS_EPOCH), tag, __ATOMIC_RELAXED, __HIP_MEMORY_SCOPE_AGENT);
    }
  }
  STAMP(7);
}

__global__ void __launch_bounds__(256) stream_ref(const u32x4* __restrict__ K, const u32x4* __restrict__ V, unsigned* out) {
  constexpr int NW = 4, DP = 4, TPW = 32;
  const int w = blockIdx.x, lane = threadIdx.x & 63, wave = __builtin_amdgcn_readfirstlane(threadIdx.x >> 6);
  const size_t base = ((size_t)(w % 16) * 2048 + (w / 16) * 128 + wave) * 256;
  const int koff = (lane & 15) * 16 + (lane >> 4);
  u32x4 kb[DP][4], vb[DP][4]; u32x4 acc = {0, 0, 0, 0};
#pragma unroll
  for (int d = 0; d < DP; ++d) {
#pragma unroll
    for (int s = 0; s < 4; ++s) kb[d][s] = __builtin_nontemporal_load(K + base + d * NW * 256 + koff + s * 4);
#pragma unroll
    for (int s = 0; s < 4; ++s) vb[d][s] = __builtin_nontemporal_load(V + base + d * NW * 256 + lane + s * 64);
    __builtin_amdgcn_sched_barrier(0);
  }
#pragma unroll
  for (int i = 0; i < TPW; ++i) {
    const int d = i % DP;
#pragma unroll
    for (int s = 0; s < 4; ++s) acc ^= kb[d][s] ^ vb[d][s];
    asm volatile("" : "+v"(acc));
    __builtin_amdgcn_sched_barrier(0);
    if (i + DP < TPW) {
#pragma unroll
      for (int s = 0; s < 4; ++s) kb[d][s] = __builtin_nontemporal_load(K + base + (i + DP) * NW * 256 + koff + s * 4);
#pragma unroll
      for (int s = 0; s < 4; ++s) vb[d][s] = __builtin_nontemporal_load(V + base + (i + DP) * NW * 256 + lane + s * 64);
    }
    __builtin_amdgcn_sched_barrier(0);
  }
  unsigned r = acc.x ^ acc.y ^ acc.z ^ acc.w;
  if (r == 0x12345678u) out[threadIdx.x] = r;
}
__global__ void tiny_k(unsigned* out) { if (out[threadIdx.x] == 0x12345679u) out[threadIdx.x + 256] = 1; }
}

extern "C" void kernel_launch(void* const* d_in, const int* in_sizes, int n_in, void* d_out, int out_size, void* d_ws, size_t ws_size,
                              hipStream_t stream) {
  Params p;
  p.x = (const unsigned short*)d_in[0];
  p.k = (const unsigned short*)d_in[1];
  p.v = (const unsigned short*)d_in[2];
  p.wq = (const unsigned short*)d_in[3];
  p.wo = (const unsigned short*)d_in[4];
  p.w1 = (const unsigned short*)d_in[6];
  p.w2 = (const unsigned short*)d_in[7];
  p.out = (unsigned short*)d_out;
  p.ws = (unsigned char*)d_ws;
  hipLaunchKernelGGL(fused_decode_kernel, dim3(NWG), dim3(NTHR), 0, stream, p);
}
```

```cpp
#define TOUCH_STRIDE 16384
#define TOUCH_N 1
#include <hip/hip_runtime.h>
#include <hip/hip_bf16.h>
#include <stdint.h>

typedef unsigned u32x4 __attribute__((ext_vector_type(4)));
typedef unsigned u32x2 __attribute__((ext_vector_type(2)));
typedef float f32x4 __attribute__((ext_vector_type(4)));
typedef float f32x2 __attribute__((ext_vector_type(2)));
typedef __bf16 bf16x2 __attribute__((ext_vector_type(2)));
typedef __bf16 bf16x8 __attribute__((ext_vector_type(8)));
typedef short s16x4 __attribute__((ext_vector_type(4)));
typedef unsigned long long u64;

namespace {

constexpr int NWG = 256, NTHR = 512, NWAVE = 8;
constexpr int SK = 32768, DH = 128, DM = 512, DFF = 1792, NQ = 4096;
constexpr int DEPTH = 6;
constexpr int NFULL = 4, REM = 2;
static_assert(4 * (NFULL * DEPTH + REM) + 4 * DEPTH == 128, "128 tiles of 16 keys per workgroup");
constexpr int NFF = DFF / 8;
constexpr unsigned WS_EPOCH = 0;
constexpr unsigned WS_QG = 4096;
constexpr unsigned WS_HP = WS_QG + 4 * 2 * NQ * 8;
constexpr unsigned WS_AP = WS_HP + 8 * 28 * 128 * 8;
constexpr unsigned WS_ST = WS_AP + NWG * 4 * 128 * 8;
constexpr unsigned WS_YP = WS_ST + 8 * 4 * 32 * 4 * 8;
constexpr unsigned WS_SP = WS_YP + 28 * 1024 * 8;
constexpr unsigned WS_END = WS_SP + 64 * 1024 * 8;
constexpr unsigned WS_DBG = 8u << 20;
constexpr unsigned SPIN_CAP = 1u << 16;
constexpr int AUX_NT = 2, AUX_SC1 = 16;
constexpr float SOFT_C = 0.08838834764831845f * 1.4426950408889634f;

__device__ __forceinline__ float bf_lo(unsigned u) { return __uint_as_float(u << 16); }
__device__ __forceinline__ float bf_hi(unsigned u) { return __uint_as_float(u & 0xffff0000u); }
__device__ __forceinline__ unsigned cvtpk(float lo, float hi) {
  return __builtin_bit_cast(unsigned, __builtin_convertvector(f32x2{lo, hi}, bf16x2));
}
__device__ __forceinline__ float rbf(float x) { return bf_lo(cvtpk(x, x)); }
template <int CTRL> __device__ __forceinline__ float dpp(float x) {
  return __builtin_bit_cast(float, __builtin_amdgcn_mov_dpp(__builtin_bit_cast(int, x), CTRL, 0xf, 0xf, true));
}
constexpr int ROR1 = 0x121, ROR2 = 0x122, ROR4 = 0x124, ROR8 = 0x128, XOR1 = 0xB1, XOR2 = 0x4E, XOR7 = 0x141;
__device__ __forceinline__ float xrow_max(float x) {
  auto s = __builtin_amdgcn_permlane16_swap(__float_as_uint(x), __float_as_uint(x), false, false);
  x = fmaxf(__uint_as_float(s[0]), __uint_as_float(s[1]));
  auto t = __builtin_amdgcn_permlane32_swap(__float_as_uint(x), __float_as_uint(x), false, false);
  return fmaxf(__uint_as_float(t[0]), __uint_as_float(t[1]));
}
__device__ __forceinline__ float xrow_sum(float x) {
  auto s = __builtin_amdgcn_permlane16_swap(__float_as_uint(x), __float_as_uint(x), false, false);
  x = __uint_as_float(s[0]) + __uint_as_float(s[1]);
  auto t = __builtin_amdgcn_permlane32_swap(__float_as_uint(x), __float_as_uint(x), false, false);
  return __uint_as_float(t[0]) + __uint_as_float(t[1]);
}
__device__ __forceinline__ float wave_sum(float x) {
  x += dpp<ROR8>(x); x += dpp<ROR4>(x); x += dpp<ROR2>(x); x += dpp<ROR1>(x);
  return xrow_sum(x);
}
__device__ __forceinline__ float tr32(float a, float b) {
  auto t = __builtin_amdgcn_permlane32_swap(__float_as_uint(a), __float_as_uint(b), false, false);
  return __uint_as_float(t[0]) + __uint_as_float(t[1]);
}
__device__ __forceinline__ float tr16(float a, float b) {
  auto t = __builtin_amdgcn_permlane16_swap(__float_as_uint(a), __float_as_uint(b), false, false);
  return __uint_as_float(t[0]) + __uint_as_float(t[1]);
}
__device__ __forceinline__ float tr8(float a, float b, bool bit3) {
  const float keep = bit3 ? b : a, send = bit3 ? a : b;
  return keep + dpp<ROR8>(send);
}
__device__ __forceinline__ float tr4(float a, float b, bool bit2) {
  const float keep = bit2 ? b : a, send = bit2 ? a : b;
  return keep + dpp<XOR7>(send);
}
__device__ __forceinline__ unsigned f24(float x) { return (__float_as_uint(x) + 0x80u) >> 8; }
__device__ __forceinline__ u32x2 gr24(unsigned payload24, unsigned tlo8, unsigned thi) { return u32x2{payload24 | tlo8, thi}; }
__device__ __forceinline__ bool ok24(unsigned lo, unsigned hi, unsigned tlo8, unsigned thi) { return ((lo & 0xff000000u) == tlo8) & (hi == thi); }
__device__ __forceinline__ float val24(unsigned lo) { return __uint_as_float(lo << 8); }

struct Params {
  const unsigned short* x; const unsigned short* k; const unsigned short* v; const unsigned short* wq; const unsigned short* wo;
  const unsigned short* w1; const unsigned short* w2; unsigned short* out; unsigned char* ws;
};

#define START_WIN 2
#define WARM_LIVE0 0
#define STAMP(i) do {} while (0)
#define LDW1(p_) LDNT(p_)
#define Q_DELAY 32
#define EARLY_GO 3
#define YP_DELAY 1
#define AUX_K 2
#define AUX_V 2
#define PRE_BAR 2
#define LDNT(p_) __builtin_nontemporal_load(reinterpret_cast<const u32x4*>(p_))

__device__ __forceinline__ void layernorm_to_lds(const u32x4 xa, const u32x4 xb, float* dst, int lane) {
  float e0[8], e1[8];
#pragma unroll
  for (int j = 0; j < 4; ++j) { e0[2 * j] = bf_lo(xa[j]); e0[2 * j + 1] = bf_hi(xa[j]); e1[2 * j] = bf_lo(xb[j]); e1[2 * j + 1] = bf_hi(xb[j]); }
  float s0 = 0.f, s1 = 0.f;
#pragma unroll
  for (int j = 0; j < 8; ++j) { s0 += e0[j]; s1 += e1[j]; }
  s0 = wave_sum(s0) * (1.f / DM); s1 = wave_sum(s1) * (1.f / DM);
  float v0 = 0.f, v1 = 0.f;
#pragma unroll
  for (int j = 0; j < 8; ++j) { const float a = e0[j] - s0, b = e1[j] - s1; v0 = fmaf(a, a, v0); v1 = fmaf(b, b, v1); }
  v0 = wave_sum(v0) * (1.f / DM); v1 = wave_sum(v1) * (1.f / DM);
  const float m0 = rbf(s0), m1 = rbf(s1);
  const float d0 = 1.f / rbf(sqrtf(rbf(rbf(v0) + 1e-5f))), d1 = 1.f / rbf(sqrtf(rbf(rbf(v1) + 1e-5f)));
#pragma unroll
  for (int j = 0; j < 8; j += 2)
    *reinterpret_cast<f32x4*>(dst + (8 * lane + j) * 2) =
        f32x4{rbf(rbf(e0[j] - m0) * d0), rbf(rbf(e1[j] - m1) * d1), rbf(rbf(e0[j + 1] - m0) * d0), rbf(rbf(e1[j + 1] - m1) * d1)};
}

__device__ __forceinline__ bool ok24x2(const u32x4 g, unsigned tlo8, unsigned thi) { return ok24(g[0], g[1], tlo8, thi) & ok24(g[2], g[3], tlo8, thi); }
__device__ __forceinline__ u32x4 gr24x2(float a, float b, unsigned tlo8, unsigned thi) { return u32x4{f24(a) | tlo8, thi, f24(b) | tlo8, thi}; }

__global__ void __launch_bounds__(NTHR, 2) fused_decode_kernel(Params p) {
  __shared__ __attribute__((aligned(16))) float lds[13312];
  float* xn_s = lds;
  float* red_q = lds + 1024;
  float* mrg_m = lds + 1536;
  float* mrg_l = lds + 1568;
  float* c_scl = lds + 1600;
  float* fin_s = lds + 1632;
  float* tr_h = lds + 1760;
  float* h_s = lds + 1888;
  float* g_s = lds + 2016;
  unsigned* q_s = reinterpret_cast<unsigned*>(lds + 2048);
  unsigned* flag_w = reinterpret_cast<unsigned*>(lds + 2304);
  float* c_acc = lds + 2560;
  float* mrg_o = lds + 4608;
  u32x4* wo_s = reinterpret_cast<u32x4*>(lds + 8704);

  const int w = blockIdx.x, tid = threadIdx.x, lane = tid & 63;
  const int wave = __builtin_amdgcn_readfirstlane(tid >> 6);
  const int kvh = w & 7, bat = (w >> 3) & 1, split = w >> 4, idx = w >> 3;
  const int n16 = lane & 15, g4 = lane >> 4, frow = lane >> 3, fcc = lane & 7;
  STAMP(0);

  const __amdgpu_buffer_rsrc_t wsr = __builtin_amdgcn_make_buffer_rsrc((void*)p.ws, 0, WS_END, 0x00020000);
#define LOAD_EPOCH() __hip_atomic_load(reinterpret_cast<const u64*>(p.ws + WS_EPOCH), __ATOMIC_RELAXED, __HIP_MEMORY_SCOPE_AGENT)
#define WG_BARRIER() do { __builtin_amdgcn_sched_barrier(0); asm volatile("s_waitcnt lgkmcnt(0)\n\ts_barrier" ::: "memory"); __builtin_amdgcn_sched_barrier(0); } while (0)
#define LDG(off_) __builtin_amdgcn_raw_buffer_load_b128(wsr, (off_), 0, AUX_SC1)
#define STG(v_, off_) __builtin_amdgcn_raw_buffer_store_b128((v_), wsr, (off_), 0, AUX_SC1)
  const bool streamer = wave >= 4;

#define SET_FLAG(i_) __hip_atomic_store(&flag_w[i_], 1u, __ATOMIC_RELEASE, __HIP_MEMORY_SCOPE_WORKGROUP)
#define GET_FLAG(i_) (__hip_atomic_load(&flag_w[i_], __ATOMIC_ACQUIRE, __HIP_MEMORY_SCOPE_WORKGROUP) != 0u)
  const __amdgpu_buffer_rsrc_t kr = __builtin_amdgcn_make_buffer_rsrc((void*)p.k, 0, 2u * 8 * SK * DH * 2, 0x00020000);
  const __amdgpu_buffer_rsrc_t vr = __builtin_amdgcn_make_buffer_rsrc((void*)p.v, 0, 2u * 8 * SK * DH * 2, 0x00020000);
  const unsigned kvoff = (unsigned)(((n16 & 3) * 4 + (n16 >> 2)) * 256 + g4 * 16);
  const unsigned vvoff = (unsigned)lane * 16;
  const unsigned sbase = (unsigned)((bat * 8 + kvh) * SK + split * 2048 + ((streamer ? 4 * DEPTH : 0) + (wave & 3)) * 16) * (DH * 2);
  constexpr unsigned TSTEP = 4 * 16 * DH * 2;
  u32x4 kb[DEPTH][4], vb[DEPTH][4];
#define ISSUE(d_, so_) do { \
    _Pragma("unroll") for (int s_ = 0; s_ < 4; ++s_) kb[d_][s_] = __builtin_amdgcn_raw_buffer_load_b128(kr, kvoff + s_ * 64, (so_), AUX_K); \
    _Pragma("unroll") for (int s_ = 0; s_ < 4; ++s_) vb[d_][s_] = __builtin_amdgcn_raw_buffer_load_b128(vr, vvoff + s_ * 1024, (so_), AUX_V); \
    __builtin_amdgcn_sched_barrier(0); } while (0)
  u64 tag;
  if (__builtin_expect(streamer, 1)) {
    STAMP(9);
#pragma unroll
    for (int d = 0; d < PRE_BAR; ++d) ISSUE(d, sbase + d * TSTEP);
    WG_BARRIER();
#pragma unroll
    for (int d = PRE_BAR; d < DEPTH; ++d) ISSUE(d, sbase + d * TSTEP);
    tag = LOAD_EPOCH() + 1;
  } else {
    unsigned tch[2 * TOUCH_N];
    {
      const unsigned wbase = (unsigned)((bat * 8 + kvh) * SK + split * 2048) * (DH * 2);
#pragma unroll
      for (int k = 0; k < TOUCH_N; ++k) {
        const unsigned o = (unsigned)((k * 4 + wave) * 64 + lane) * TOUCH_STRIDE;
        const unsigned so = o < 2048u * DH * 2 ? wbase : 0x80000000u;
        tch[2 * k] = __builtin_amdgcn_raw_buffer_load_b32(kr, o < 2048u * DH * 2 ? wbase + o : 0x80000000u, 0, AUX_K);
        tch[2 * k + 1] = __builtin_amdgcn_raw_buffer_load_b32(vr, o < 2048u * DH * 2 ? wbase + o : 0x80000000u, 0, AUX_V);
        (void)so;
      }
    }
    const u32x4 xa = *reinterpret_cast<const u32x4*>(p.x + 8 * lane);
    const u32x4 xb = *reinterpret_cast<const u32x4*>(p.x + DM + 8 * lane);
    tag = LOAD_EPOCH() + 1;
    __builtin_amdgcn_sched_barrier(0);
    const int qrow = (w >> 6) * 128 + wave * 32 + frow;
    const unsigned short* wqp = p.wq + (size_t)qrow * NQ + (w & 63) * 64 + fcc * 8;
    u32x4 wv[4];
#pragma unroll
    for (int k = 0; k < 4; ++k) wv[k] = LDNT(wqp + (size_t)k * 8 * NQ);
    if (tid < 16) flag_w[tid] = 0u;
    WG_BARRIER();
#pragma unroll
    for (int k = 0; k < 2 * TOUCH_N; ++k) asm volatile("" :: "v"(tch[k]));
    layernorm_to_lds(xa, xb, xn_s, lane);
    const unsigned t40_lo = (unsigned)(tag << 24), t40_hi = (unsigned)(tag >> 8);
    const bool b3 = (lane >> 3) & 1;
    {
      float qa[16];
#pragma unroll
      for (int j = 0; j < 16; ++j) qa[j] = 0.f;
#pragma unroll
      for (int k = 0; k < 4; ++k) {
        const f32x2 xv = *reinterpret_cast<const f32x2*>(xn_s + (qrow + 8 * k) * 2);
#pragma unroll
        for (int j = 0; j < 4; ++j) {
          const float l0 = bf_lo(wv[k][j]), h0 = bf_hi(wv[k][j]);
          qa[2 * j] = fmaf(xv[0], l0, qa[2 * j]); qa[2 * j + 1] = fmaf(xv[0], h0, qa[2 * j + 1]);
          qa[8 + 2 * j] = fmaf(xv[1], l0, qa[8 + 2 * j]); qa[8 + 2 * j + 1] = fmaf(xv[1], h0, qa[8 + 2 * j + 1]);
        }
      }
#pragma unroll
      for (int j = 0; j < 8; ++j) qa[j] = tr32(qa[j], qa[j + 8]);
#pragma unroll
      for (int j = 0; j < 4; ++j) qa[j] = tr16(qa[j], qa[j + 4]);
      *reinterpret_cast<f32x2*>(red_q + wave * 128 + (lane >> 5) * 64 + fcc * 8 + ((lane >> 3) & 3) * 2) = f32x2{tr8(qa[0], qa[2], b3), tr8(qa[1], qa[3], b3)};
      SET_FLAG(wave);
    }
    u32x4 w1v[8];
    const bool has_f1 = w < NFF;
    const int f1row = (w / 28) * 64 + frow;
    if (wave == 0) {
      while (!__all(GET_FLAG(lane & 3))) __builtin_amdgcn_s_sleep(0);
      f32x2 t = *reinterpret_cast<const f32x2*>(red_q + 2 * lane);
#pragma unroll
      for (int v = 1; v < 4; ++v) t += *reinterpret_cast<const f32x2*>(red_q + v * 128 + 2 * lane);
      STG(gr24x2(t[0], t[1], t40_lo, t40_hi), WS_QG + (unsigned)(((w >> 6) * 2 + (lane >> 5)) * NQ + (w & 63) * 64 + (lane & 31) * 2) * 8);
      STAMP(1);
    } else if (wave == 1 && has_f1) {
#pragma unroll
      for (int k = 0; k < 8; ++k) w1v[k] = LDNT(p.w1 + (size_t)(f1row + 8 * k) * DFF + (w % 28) * 64 + fcc * 8);
    }
    __builtin_amdgcn_sched_barrier(0);
    {
      const unsigned qoff = WS_QG + (unsigned)(bat * NQ + kvh * 512 + wave * 128 + 2 * lane) * 8;
      u32x4 qg[4];
      unsigned spins = 0;
      __builtin_amdgcn_s_sleep(Q_DELAY);
#pragma unroll
      for (int k = 0; k < 4; ++k) qg[k] = LDG(qoff + k * (2 * NQ * 8));
      for (;;) {
        const bool k0 = __all(ok24x2(qg[0], t40_lo, t40_hi)), k1 = __all(ok24x2(qg[1], t40_lo, t40_hi));
        const bool k2 = __all(ok24x2(qg[2], t40_lo, t40_hi)), k3 = __all(ok24x2(qg[3], t40_lo, t40_hi));
        if ((k0 & k1 & k2 & k3) || ++spins >= SPIN_CAP) break;
        __builtin_amdgcn_s_sleep(8);
        asm volatile("" ::: "memory");
        if (!k0) qg[0] = LDG(qoff);
        if (!k1) qg[1] = LDG(qoff + 1 * (2 * NQ * 8));
        if (!k2) qg[2] = LDG(qoff + 2 * (2 * NQ * 8));
        if (!k3) qg[3] = LDG(qoff + 3 * (2 * NQ * 8));
      }
      q_s[wave * 64 + lane] = cvtpk((val24(qg[0][0]) + val24(qg[1][0])) + (val24(qg[2][0]) + val24(qg[3][0])),
                                    (val24(qg[0][2]) + val24(qg[1][2])) + (val24(qg[2][2]) + val24(qg[3][2])));
      SET_FLAG(4 + wave);
      STAMP(11);
    }
    if (wave == 1 && has_f1) {
      float ha[16];
#pragma unroll
      for (int j = 0; j < 16; ++j) ha[j] = 0.f;
#pragma unroll
      for (int k = 0; k < 8; ++k) {
        const f32x2 xv = *reinterpret_cast<const f32x2*>(xn_s + (f1row + 8 * k) * 2);
#pragma unroll
        for (int j = 0; j < 4; ++j) {
          const float l0 = bf_lo(w1v[k][j]), h0 = bf_hi(w1v[k][j]);
          ha[2 * j] = fmaf(xv[0], l0, ha[2 * j]); ha[2 * j + 1] = fmaf(xv[0], h0, ha[2 * j + 1]);
          ha[8 + 2 * j] = fmaf(xv[1], l0, ha[8 + 2 * j]); ha[8 + 2 * j + 1] = fmaf(xv[1], h0, ha[8 + 2 * j + 1]);
        }
      }
#pragma unroll
      for (int j = 0; j < 8; ++j) ha[j] = tr32(ha[j], ha[j + 8]);
#pragma unroll
      for (int j = 0; j < 4; ++j) ha[j] = tr16(ha[j], ha[j + 4]);
      { const int u = fcc * 8 + (frow & 3) * 2;
        tr_h[u * 2 + (lane >> 5)] = tr8(ha[0], ha[2], b3); tr_h[u * 2 + 2 + (lane >> 5)] = tr8(ha[1], ha[3], b3); }
      const f32x2 t = *reinterpret_cast<const f32x2*>(tr_h + 2 * lane);
      STG(gr24x2(t[0], t[1], t40_lo, t40_hi), WS_HP + (unsigned)(w * 128 + 2 * lane) * 8);
      STAMP(10);
    } else if (wave == 2) {
      u32x4 t[16];
#pragma unroll
      for (int k = 0; k < 16; ++k) t[k] = LDNT(p.wo + (size_t)(kvh * 512 + (idx >> 2) * 64 + k * 4 + g4) * DM + (idx & 3) * 128 + n16 * 8);
#pragma unroll
      for (int k = 0; k < 16; ++k) wo_s[(k * 4 + g4) * 17 + n16] = t[k];
    }
    __builtin_amdgcn_sched_barrier(0);
#pragma unroll
    for (int d = 0; d < DEPTH; ++d) ISSUE(d, sbase + d * TSTEP);
  }

  {
    const unsigned msk0 = (n16 & 3) == 0 ? 0x0000ffffu : (n16 & 3) == 1 ? 0xffff0000u : 0u;
    const unsigned msk1 = (n16 & 3) == 2 ? 0x0000ffffu : (n16 & 3) == 3 ? 0xffff0000u : 0u;
    float m_run = -INFINITY, l_run = 0.f;
    f32x4 olo = {0.f, 0.f, 0.f, 0.f}, ohi = {0.f, 0.f, 0.f, 0.f};
    while (!__all(GET_FLAG(4 + (lane & 3)))) __builtin_amdgcn_s_sleep(1);
    bf16x8 qf[4];
#pragma unroll
    for (int s = 0; s < 4; ++s) qf[s] = *reinterpret_cast<const bf16x8*>(q_s + ((n16 >> 2) * DH + s * 32 + g4 * 8) / 2);
    STAMP(2);
#define TILE(d) do { \
      f32x4 sc = {0.f, 0.f, 0.f, 0.f}; \
      _Pragma("unroll") for (int s = 0; s < 4; ++s) sc = __builtin_amdgcn_mfma_f32_16x16x32_bf16(__builtin_bit_cast(bf16x8, kb[d][s]), qf[s], sc, 0, 0, 0); \
      float tmax = fmaxf(fmaxf(sc[0], sc[1]), fmaxf(sc[2], sc[3])); \
      tmax = xrow_max(tmax); \
      const float mnew = fmaxf(m_run, tmax); \
      const float alpha = __builtin_amdgcn_exp2f((m_run - mnew) * SOFT_C); \
      const float mc = -mnew * SOFT_C; \
      float pr[4]; \
      _Pragma("unroll") for (int r = 0; r < 4; ++r) pr[r] = __builtin_amdgcn_exp2f(fmaf(sc[r], SOFT_C, mc)); \
      l_run = fmaf(l_run, alpha, (pr[0] + pr[1]) + (pr[2] + pr[3])); \
      m_run = mnew; \
      olo *= alpha; ohi *= alpha; \
      _Pragma("unroll") for (int r = 0; r < 4; ++r) { \
        const unsigned pk = cvtpk(pr[r], pr[r]); \
        const u32x2 bp = {pk & msk0, pk & msk1}; \
        const u32x2 vlo = {vb[d][r][0], vb[d][r][1]}, vhi = {vb[d][r][2], vb[d][r][3]}; \
        olo = __builtin_amdgcn_mfma_f32_16x16x16bf16_1k(__builtin_bit_cast(s16x4, vlo), __builtin_bit_cast(s16x4, bp), olo, 0, 0, 0); \
        ohi = __builtin_amdgcn_mfma_f32_16x16x16bf16_1k(__builtin_bit_cast(s16x4, vhi), __builtin_bit_cast(s16x4, bp), ohi, 0, 0, 0); \
      } \
      __builtin_amdgcn_sched_barrier(0); } while (0)
    if (streamer) {
#pragma unroll
      for (int d = 0; d < REM; ++d) { TILE(d); ISSUE(d, sbase + (unsigned)(DEPTH + d) * TSTEP); }
#pragma unroll 1
      for (int it = 0; it < NFULL - 1; ++it) {
        const unsigned snext = sbase + (unsigned)(REM + (it + 1) * DEPTH) * TSTEP;
#pragma unroll
        for (int d = 0; d < DEPTH; ++d) {
          TILE((d + REM) % DEPTH);
          ISSUE((d + REM) % DEPTH, snext + d * TSTEP);
        }
      }
#pragma unroll
      for (int d = 0; d < DEPTH; ++d) TILE((d + REM) % DEPTH);
    } else {
#pragma unroll
      for (int d = 0; d < DEPTH; ++d) TILE(d);
    }
#undef TILE
#undef ISSUE
    STAMP(3);
    l_run = xrow_sum(l_run);
    float* mo = mrg_o + wave * 512 + (n16 >> 2) * DH + g4 * 32 + (n16 & 3);
#pragma unroll
    for (int r = 0; r < 4; ++r) { mo[r * 8] = olo[r]; mo[r * 8 + 4] = ohi[r]; }
    if (g4 == 0 && (n16 & 3) == 0) { mrg_m[wave * 4 + (n16 >> 2)] = m_run; mrg_l[wave * 4 + (n16 >> 2)] = l_run; }
  }
  const unsigned t40_lo = (unsigned)(tag << 24), t40_hi = (unsigned)(tag >> 8);
  if (wave == 3) {
    const bool b3 = (lane >> 3) & 1;
    if (w < NFF) {
      u32x4 w2v[8];
#pragma unroll
      for (int k = 0; k < 8; ++k) w2v[k] = LDNT(p.w2 + (size_t)((w >> 3) * 64 + frow + 8 * k) * DM + (w & 7) * 64 + fcc * 8);
      const unsigned hoff = WS_HP + (unsigned)((w >> 3) * 128 + 2 * lane) * 8;
      u32x4 hg[8];
      unsigned spins = 0;
      for (;;) {
        bool ok = true;
#pragma unroll
        for (int k = 0; k < 8; ++k) hg[k] = LDG(hoff + k * (28 * 128 * 8));
#pragma unroll
        for (int k = 0; k < 8; ++k) ok &= ok24x2(hg[k], t40_lo, t40_hi);
        if (__all(ok) || ++spins >= SPIN_CAP) break;
        __builtin_amdgcn_s_sleep(32);
        asm volatile("" ::: "memory");
      }
      float h0 = 0.f, h1 = 0.f;
#pragma unroll
      for (int k = 0; k < 8; ++k) { h0 += val24(hg[k][0]); h1 += val24(hg[k][2]); }
      *reinterpret_cast<f32x2*>(h_s + 2 * lane) = f32x2{fmaxf(rbf(h0), 0.f), fmaxf(rbf(h1), 0.f)};
      float ya[16];
#pragma unroll
      for (int j = 0; j < 16; ++j) ya[j] = 0.f;
#pragma unroll
      for (int k = 0; k < 8; ++k) {
        const f32x2 hv = *reinterpret_cast<const f32x2*>(h_s + (frow + 8 * k) * 2);
#pragma unroll
        for (int j = 0; j < 4; ++j) {
          const float l0 = bf_lo(w2v[k][j]), hh = bf_hi(w2v[k][j]);
          ya[2 * j] = fmaf(hv[0], l0, ya[2 * j]); ya[2 * j + 1] = fmaf(hv[0], hh, ya[2 * j + 1]);
          ya[8 + 2 * j] = fmaf(hv[1], l0, ya[8 + 2 * j]); ya[8 + 2 * j + 1] = fmaf(hv[1], hh, ya[8 + 2 * j + 1]);
        }
      }
#pragma unroll
      for (int j = 0; j < 8; ++j) ya[j] = tr32(ya[j], ya[j + 8]);
#pragma unroll
      for (int j = 0; j < 4; ++j) ya[j] = tr16(ya[j], ya[j + 4]);
      asm volatile("" ::: "memory");
      *reinterpret_cast<f32x2*>(h_s + ((fcc >> 1) * 2 + (lane >> 5)) * 16 + ((frow & 3) * 2 + (fcc & 1)) * 2) = f32x2{tr8(ya[0], ya[2], b3), tr8(ya[1], ya[3], b3)};
      const f32x2 t = *reinterpret_cast<const f32x2*>(h_s + 2 * lane);
      STG(gr24x2(t[0], t[1], t40_lo, t40_hi), WS_YP + (unsigned)((w >> 3) * 1024 + (w & 7) * 128 + 2 * lane) * 8);
    }
    STAMP(8);
    if (w < 64) {
      const unsigned yoff = WS_YP + (unsigned)(frow * 1024 + w * 16 + fcc * 2) * 8;
      u32x4 yg[4];
      unsigned spins = 0;
      for (;;) {
        __builtin_amdgcn_s_sleep(64);
        bool ok = true;
#pragma unroll
        for (int k = 0; k < 4; ++k) yg[k] = (k < 3 || lane < 32) ? LDG(yoff + k * 8 * 8192) : u32x4{t40_lo, t40_hi, t40_lo, t40_hi};
#pragma unroll
        for (int k = 0; k < 4; ++k) ok &= ok24x2(yg[k], t40_lo, t40_hi);
        if (__all(ok) || ++spins >= SPIN_CAP) break;
        asm volatile("" ::: "memory");
      }
      float y0 = (val24(yg[0][0]) + val24(yg[1][0])) + (val24(yg[2][0]) + val24(yg[3][0]));
      float y1 = (val24(yg[0][2]) + val24(yg[1][2])) + (val24(yg[2][2]) + val24(yg[3][2]));
      y0 += dpp<ROR8>(y0); y0 = rbf(xrow_sum(y0));
      y1 += dpp<ROR8>(y1); y1 = rbf(xrow_sum(y1));
      if (lane < 8) *reinterpret_cast<f32x2*>(g_s + lane * 2) = f32x2{rbf(y0 / (1.f + __expf(-y0))), rbf(y1 / (1.f + __expf(-y1)))};
    }
    STAMP(12);
  }
  asm volatile("" :: "v"(t40_lo), "v"(t40_hi));
  __syncthreads();
  {
    const int h = tid >> 7;
    float mw[8], M = -INFINITY;
#pragma unroll
    for (int v = 0; v < 8; ++v) { mw[v] = mrg_m[v * 4 + h]; M = fmaxf(M, mw[v]); }
    float o = 0.f, L = 0.f;
#pragma unroll
    for (int v = 0; v < 8; ++v) {
      const float f = __builtin_amdgcn_exp2f((mw[v] - M) * SOFT_C);
      o = fmaf(f, mrg_o[v * 512 + tid], o); L = fmaf(f, mrg_l[v * 4 + h], L);
    }
    __builtin_amdgcn_raw_buffer_store_b64(gr24(f24(o), t40_lo, t40_hi), wsr, WS_AP + (unsigned)(w * 512 + tid) * 8, 0, AUX_SC1);
    if ((tid & 127) < 2) {
      const unsigned mb = __float_as_uint(M);
      const unsigned p0 = (tid & 1) ? f24(L) : (mb >> 8), p1 = (tid & 1) ? 0u : (mb & 0xffu);
      STG((u32x4{p0 | t40_lo, t40_hi, p1 | t40_lo, t40_hi}), WS_ST + (unsigned)(((kvh * 4 + h) * 32 + bat * 16 + split) * 4 + 2 * (tid & 1)) * 8);
    }
  }
  STAMP(4);
  float wof[16];
  {
    const u32x4 woa = wo_s[(lane >> 1) * 17 + wave * 2 + (lane & 1)], wob = wo_s[(32 + (lane >> 1)) * 17 + wave * 2 + (lane & 1)];
#pragma unroll
    for (int j = 0; j < 4; ++j) { wof[2 * j] = bf_lo(woa[j]); wof[2 * j + 1] = bf_hi(woa[j]); wof[8 + 2 * j] = bf_lo(wob[j]); wof[8 + 2 * j + 1] = bf_hi(wob[j]); }
  }
  {
    if (wave == 0) {
      const unsigned o2 = WS_ST + (unsigned)((kvh * 4 + (idx >> 3)) * 128 + lane * 2) * 8;
      u32x4 gs;
      unsigned spins = 0;
      for (;;) {
        gs = LDG(o2);
        const int missing = __popcll(__ballot(!ok24x2(gs, t40_lo, t40_hi)));
        if (missing <= 2 * EARLY_GO) SET_FLAG(8);
        if (missing == 0 || ++spins >= SPIN_CAP) break;
        asm volatile("" ::: "memory");
      }
      SET_FLAG(8);
      const float mine = __uint_as_float((gs[0] << 8) | ((lane & 1) ? 0u : (gs[2] & 0xffu)));
      const float other = dpp<XOR1>(mine);
      const float m = (lane & 1) ? other : mine, l = (lane & 1) ? mine : other;
      float M = m; M = fmaxf(M, dpp<XOR2>(M)); M = fmaxf(M, dpp<XOR7>(M)); M = fmaxf(M, dpp<ROR8>(M));
      { auto t = __builtin_amdgcn_permlane16_swap(__float_as_uint(M), __float_as_uint(M), false, false); M = fmaxf(__uint_as_float(t[0]), __uint_as_float(t[1])); }
      const float f = __builtin_amdgcn_exp2f((m - M) * SOFT_C);
      float L = f * l; L += dpp<XOR2>(L); L += dpp<XOR7>(L); L += dpp<ROR8>(L); L = tr16(L, L);
      if ((lane & 1) == 0) c_scl[lane >> 1] = f / L;
    } else {
      while (!GET_FLAG(8)) __builtin_amdgcn_s_sleep(0);
    }
    const int pp0 = wave * 4 + (lane >> 5), pp1 = pp0 + 2;
    const unsigned o1 = WS_AP + (unsigned)((kvh + 8 * (pp0 >> 4) + 16 * (pp0 & 15)) * 512 + (idx >> 2) * 64 + (lane & 31) * 2) * 8;
    u32x4 ga, gb;
    unsigned spins = 0;
    for (;;) {
      ga = LDG(o1); gb = LDG(o1 + 2 * 16 * 512 * 8);
      if (__all(ok24x2(ga, t40_lo, t40_hi) & ok24x2(gb, t40_lo, t40_hi)) || ++spins >= SPIN_CAP) break;
      asm volatile("" ::: "memory");
    }
    *reinterpret_cast<f32x2*>(c_acc + pp0 * 64 + (lane & 31) * 2) = f32x2{val24(ga[0]), val24(ga[2])};
    *reinterpret_cast<f32x2*>(c_acc + pp1 * 64 + (lane & 31) * 2) = f32x2{val24(gb[0]), val24(gb[2])};
  }
  __syncthreads();
  STAMP(5);
  {
    float o0 = 0.f, o1 = 0.f;
#pragma unroll
    for (int s4 = 0; s4 < 4; ++s4) {
      const f32x4 sa = *reinterpret_cast<const f32x4*>(c_scl + s4 * 4), sb = *reinterpret_cast<const f32x4*>(c_scl + 16 + s4 * 4);
#pragma unroll
      for (int j = 0; j < 4; ++j) { o0 = fmaf(sa[j], c_acc[(s4 * 4 + j) * 64 + lane], o0); o1 = fmaf(sb[j], c_acc[(16 + s4 * 4 + j) * 64 + lane], o1); }
    }
    o0 = rbf(o0); o1 = rbf(o1);
    const float o00 = __shfl(o0, lane >> 1), o01 = __shfl(o0, 32 + (lane >> 1)), o10 = __shfl(o1, lane >> 1), o11 = __shfl(o1, 32 + (lane >> 1));
    float sa[8];
#pragma unroll
    for (int j = 0; j < 8; ++j) sa[j] = tr32(fmaf(o01, wof[8 + j], o00 * wof[j]), fmaf(o11, wof[8 + j], o10 * wof[j]));
#pragma unroll
    for (int j = 0; j < 4; ++j) sa[j] = tr16(sa[j], sa[j + 4]);
    const bool b1 = (lane >> 1) & 1;
    float r0 = (b1 ? sa[2] : sa[0]) + dpp<XOR2>(b1 ? sa[0] : sa[2]), r1 = (b1 ? sa[3] : sa[1]) + dpp<XOR2>(b1 ? sa[1] : sa[3]);
    r0 += dpp<ROR4>(r0); r0 += dpp<ROR8>(r0);
    r1 += dpp<ROR4>(r1); r1 += dpp<ROR8>(r1);
    if ((lane & 12) == 0)
      STG(gr24x2(r0, r1, t40_lo, t40_hi),
          WS_SP + (unsigned)((kvh * 8 + (idx >> 2)) * 1024 + (((idx & 3) * 8 + wave) * 2 + (lane >> 5)) * 16 + (((lane >> 4) & 1) * 4 + (lane & 3)) * 2) * 8);
  }
  STAMP(6);
  if (w < 64) {
    const unsigned soff = WS_SP + (unsigned)((wave * 8 + frow) * 1024 + w * 16 + fcc * 2) * 8;
    const int oidx = (w & 1) * DM + (w >> 1) * 16 + (fcc & 1) * 8 + (fcc >> 2) * 4 + ((fcc >> 1) & 1) * 2;
    const unsigned xr = *reinterpret_cast<const unsigned*>(p.x + oidx);
    u32x4 sg;
    unsigned spins = 0;
    for (;;) {
      sg = LDG(soff);
      if (__all(ok24x2(sg, t40_lo, t40_hi)) || ++spins >= SPIN_CAP) break;
      asm volatile("" ::: "memory");
    }
    float s0 = val24(sg[0]), s1 = val24(sg[2]);
    s0 += dpp<ROR8>(s0); s0 = xrow_sum(s0);
    s1 += dpp<ROR8>(s1); s1 = xrow_sum(s1);
    if (lane < 8) *reinterpret_cast<f32x2*>(fin_s + wave * 16 + lane * 2) = f32x2{s0, s1};
    __syncthreads();
    if (wave == 0 && lane < 8) {
      float sa = 0.f, sb = 0.f;
#pragma unroll
      for (int v = 0; v < 8; ++v) { const f32x2 t = *reinterpret_cast<const f32x2*>(fin_s + v * 16 + lane * 2); sa += t[0]; sb += t[1]; }
      const f32x2 g = *reinterpret_cast<const f32x2*>(g_s + lane * 2);
      const float r0 = bf_lo(xr) + rbf(rbf(sa) * g[0]), r1 = bf_hi(xr) + rbf(rbf(sb) * g[1]);
      *reinterpret_cast<unsigned*>(p.out + oidx) = cvtpk(r0, r1);
      if (w == 0 && lane == 0) __hip_atomic_store(reinterpret_cast<u64*>(p.ws + WS_EPOCH), tag, __ATOMIC_RELAXED, __HIP_MEMORY_SCOPE_AGENT);
    }
  }
  STAMP(7);
}

}

extern "C" void kernel_launch(void* const* d_in, const int* in_sizes, int n_in, void* d_out, int out_size, void* d_ws, size_t ws_size,
                              hipStream_t stream) {
  Params p;
  p.x = (const unsigned short*)d_in[0];
  p.k = (const unsigned short*)d_in[1];
  p.v = (const unsigned short*)d_in[2];
  p.wq = (const unsigned short*)d_in[3];
  p.wo = (const unsigned short*)d_in[4];
  p.w1 = (const unsigned short*)d_in[6];
  p.w2 = (const unsigned short*)d_in[7];
  p.out = (unsigned short*)d_out;
  p.ws = (unsigned char*)d_ws;
  hipLaunchKernelGGL(fused_decode_kernel, dim3(NWG), dim3(NTHR), 0, stream, p);
}
```

```cpp
#include <hip/hip_runtime.h>
#include <hip/hip_bf16.h>
#include <stdint.h>

typedef unsigned u32x4 __attribute__((ext_vector_type(4)));
typedef unsigned u32x2 __attribute__((ext_vector_type(2)));
typedef float f32x4 __attribute__((ext_vector_type(4)));
typedef float f32x2 __attribute__((ext_vector_type(2)));
typedef __bf16 bf16x2 __attribute__((ext_vector_type(2)));
typedef __bf16 bf16x8 __attribute__((ext_vector_type(8)));
typedef short s16x4 __attribute__((ext_vector_type(4)));
typedef unsigned long long u64;

namespace {

constexpr int NWG = 256, NTHR = 512, NWAVE = 8;
constexpr int SK = 32768, DH = 128, DM = 512, DFF = 1792, NQ = 4096;
constexpr int DEPTH = 6;
constexpr int NFULL = 4, REM = 2;
static_assert(4 * (NFULL * DEPTH + REM) + 4 * DEPTH == 128, "128 tiles of 16 keys per workgroup");
constexpr int NFF = DFF / 8;
constexpr unsigned WS_EPOCH = 0;
constexpr unsigned WS_QG = 4096;
constexpr unsigned WS_HP = WS_QG + 4 * 2 * NQ * 8;
constexpr unsigned WS_AP = WS_HP + 8 * 28 * 128 * 8;
constexpr unsigned WS_ST = WS_AP + NWG * 4 * 128 * 8;
constexpr unsigned WS_YP = WS_ST + 8 * 4 * 32 * 4 * 8;
constexpr unsigned WS_SP = WS_YP + 28 * 1024 * 8;
constexpr unsigned WS_END = WS_SP + 64 * 1024 * 8;
constexpr unsigned WS_DBG = 8u << 20;
constexpr unsigned SPIN_CAP = 1u << 16;
constexpr int AUX_NT = 2, AUX_SC1 = 16;
constexpr float SOFT_C = 0.08838834764831845f * 1.4426950408889634f;

__device__ __forceinline__ float bf_lo(unsigned u) { return __uint_as_float(u << 16); }
__device__ __forceinline__ float bf_hi(unsigned u) { return __uint_as_float(u & 0xffff0000u); }
__device__ __forceinline__ unsigned cvtpk(float lo, float hi) {
  return __builtin_bit_cast(unsigned, __builtin_convertvector(f32x2{lo, hi}, bf16x2));
}
__device__ __forceinline__ float rbf(float x) { return bf_lo(cvtpk(x, x)); }
template <int CTRL> __device__ __forceinline__ float dpp(float x) {
  return __builtin_bit_cast(float, __builtin_amdgcn_mov_dpp(__builtin_bit_cast(int, x), CTRL, 0xf, 0xf, true));
}
constexpr int ROR1 = 0x121, ROR2 = 0x122, ROR4 = 0x124, ROR8 = 0x128, XOR1 = 0xB1, XOR2 = 0x4E, XOR7 = 0x141;
__device__ __forceinline__ float xrow_max(float x) {
  auto s = __builtin_amdgcn_permlane16_swap(__float_as_uint(x), __float_as_uint(x), false, false);
  x = fmaxf(__uint_as_float(s[0]), __uint_as_float(s[1]));
  auto t = __builtin_amdgcn_permlane32_swap(__float_as_uint(x), __float_as_uint(x), false, false);
  return fmaxf(__uint_as_float(t[0]), __uint_as_float(t[1]));
}
__device__ __forceinline__ float xrow_sum(float x) {
  auto s = __builtin_amdgcn_permlane16_swap(__float_as_uint(x), __float_as_uint(x), false, false);
  x = __uint_as_float(s[0]) + __uint_as_float(s[1]);
  auto t = __builtin_amdgcn_permlane32_swap(__float_as_uint(x), __float_as_uint(x), false, false);
  return __uint_as_float(t[0]) + __uint_as_float(t[1]);
}
__device__ __forceinline__ float wave_sum(float x) {
  x += dpp<ROR8>(x); x += dpp<ROR4>(x); x += dpp<ROR2>(x); x += dpp<ROR1>(x);
  return xrow_sum(x);
}
__device__ __forceinline__ float tr32(float a, float b) {
  auto t = __builtin_amdgcn_permlane32_swap(__float_as_uint(a), __float_as_uint(b), false, false);
  return __uint_as_float(t[0]) + __uint_as_float(t[1]);
}
__device__ __forceinline__ float tr16(float a, float b) {
  auto t = __builtin_amdgcn_permlane16_swap(__float_as_uint(a), __float_as_uint(b), false, false);
  return __uint_as_float(t[0]) + __uint_as_float(t[1]);
}
__device__ __forceinline__ float tr8(float a, float b, bool bit3) {
  const float keep = bit3 ? b : a, send = bit3 ? a : b;
  return keep + dpp<ROR8>(send);
}
__device__ __forceinline__ float tr4(float a, float b, bool bit2) {
  const float keep = bit2 ? b : a, send = bit2 ? a : b;
  return keep + dpp<XOR7>(send);
}
__device__ __forceinline__ unsigned f24(float x) { return (__float_as_uint(x) + 0x80u) >> 8; }
__device__ __forceinline__ u32x2 gr24(unsigned payload24, unsigned tlo8, unsigned thi) { return u32x2{payload24 | tlo8, thi}; }
__device__ __forceinline__ bool ok24(unsigned lo, unsigned hi, unsigned tlo8, unsigned thi) { return ((lo & 0xff000000u) == tlo8) & (hi == thi); }
__device__ __forceinline__ float val24(unsigned lo) { return __uint_as_float(lo << 8); }

struct Params {
  const unsigned short* x; const unsigned short* k; const unsigned short* v; const unsigned short* wq; const unsigned short* wo;
  const unsigned short* w1; const unsigned short* w2; unsigned short* out; unsigned char* ws;
};

#define START_WIN 2
#define WARM_LIVE0 0
#define STAMP(i) do {} while (0)
#define LDW1(p_) LDNT(p_)
#define Q_DELAY 32
#define EARLY_GO 3
#define YP_DELAY 1
#define AUX_K 2
#define AUX_V 2
#define PRE_BAR 2
#define LDNT(p_) __builtin_nontemporal_load(reinterpret_cast<const u32x4*>(p_))

__device__ __forceinline__ void layernorm_to_lds(const u32x4 xa, const u32x4 xb, float* dst, int lane) {
  float e0[8], e1[8];
#pragma unroll
  for (int j = 0; j < 4; ++j) { e0[2 * j] = bf_lo(xa[j]); e0[2 * j + 1] = bf_hi(xa[j]); e1[2 * j] = bf_lo(xb[j]); e1[2 * j + 1] = bf_hi(xb[j]); }
  float s0 = 0.f, s1 = 0.f;
#pragma unroll
  for (int j = 0; j < 8; ++j) { s0 += e0[j]; s1 += e1[j]; }
  s0 = wave_sum(s0) * (1.f / DM); s1 = wave_sum(s1) * (1.f / DM);
  float v0 = 0.f, v1 = 0.f;
#pragma unroll
  for (int j = 0; j < 8; ++j) { const float a = e0[j] - s0, b = e1[j] - s1; v0 = fmaf(a, a, v0); v1 = fmaf(b, b, v1); }
  v0 = wave_sum(v0) * (1.f / DM); v1 = wave_sum(v1) * (1.f / DM);
  const float m0 = rbf(s0), m1 = rbf(s1);
  const float d0 = 1.f / rbf(sqrtf(rbf(rbf(v0) + 1e-5f))), d1 = 1.f / rbf(sqrtf(rbf(rbf(v1) + 1e-5f)));
#pragma unroll
  for (int j = 0; j < 8; j += 2)
    *reinterpret_cast<f32x4*>(dst + (8 * lane + j) * 2) =
        f32x4{rbf(rbf(e0[j] - m0) * d0), rbf(rbf(e1[j] - m1) * d1), rbf(rbf(e0[j + 1] - m0) * d0), rbf(rbf(e1[j + 1] - m1) * d1)};
}

__device__ __forceinline__ bool ok24x2(const u32x4 g, unsigned tlo8, unsigned thi) { return ok24(g[0], g[1], tlo8, thi) & ok24(g[2], g[3], tlo8, thi); }
__device__ __forceinline__ u32x4 gr24x2(float a, float b, unsigned tlo8, unsigned thi) { return u32x4{f24(a) | tlo8, thi, f24(b) | tlo8, thi}; }

__global__ void __launch_bounds__(NTHR, 2) fused_decode_kernel(Params p) {
  __shared__ __attribute__((aligned(16))) float lds[13312];
  float* xn_s = lds;
  float* red_q = lds + 1024;
  float* mrg_m = lds + 1536;
  float* mrg_l = lds + 1568;
  float* c_scl = lds + 1600;
  float* fin_s = lds + 1632;
  float* tr_h = lds + 1760;
  float* h_s = lds + 1888;
  float* g_s = lds + 2016;
  unsigned* q_s = reinterpret_cast<unsigned*>(lds + 2048);
  unsigned* flag_w = reinterpret_cast<unsigned*>(lds + 2304);
  float* c_acc = lds + 2560;
  float* mrg_o = lds + 4608;
  u32x4* wo_s = reinterpret_cast<u32x4*>(lds + 8704);

  const int w = blockIdx.x, tid = threadIdx.x, lane = tid & 63;
  const int wave = __builtin_amdgcn_readfirstlane(tid >> 6);
  const int kvh = w & 7, bat = (w >> 3) & 1, split = w >> 4, idx = w >> 3;
  const int n16 = lane & 15, g4 = lane >> 4, frow = lane >> 3, fcc = lane & 7;
  STAMP(0);

  const __amdgpu_buffer_rsrc_t wsr = __builtin_amdgcn_make_buffer_rsrc((void*)p.ws, 0, WS_END, 0x00020000);
#define LOAD_EPOCH() __hip_atomic_load(reinterpret_cast<const u64*>(p.ws + WS_EPOCH), __ATOMIC_RELAXED, __HIP_MEMORY_SCOPE_AGENT)
#define WG_BARRIER() do { __builtin_amdgcn_sched_barrier(0); asm volatile("s_waitcnt lgkmcnt(0)\n\ts_barrier" ::: "memory"); __builtin_amdgcn_sched_barrier(0); } while (0)
#define LDG(off_) __builtin_amdgcn_raw_buffer_load_b128(wsr, (off_), 0, AUX_SC1)
#define STG(v_, off_) __builtin_amdgcn_raw_buffer_store_b128((v_), wsr, (off_), 0, AUX_SC1)
  const bool streamer = wave >= 4;

#define SET_FLAG(i_) __hip_atomic_store(&flag_w[i_], 1u, __ATOMIC_RELEASE, __HIP_MEMORY_SCOPE_WORKGROUP)
#define GET_FLAG(i_) (__hip_atomic_load(&flag_w[i_], __ATOMIC_ACQUIRE, __HIP_MEMORY_SCOPE_WORKGROUP) != 0u)
  const __amdgpu_buffer_rsrc_t kr = __builtin_amdgcn_make_buffer_rsrc((void*)p.k, 0, 2u * 8 * SK * DH * 2, 0x00020000);
  const __amdgpu_buffer_rsrc_t vr = __builtin_amdgcn_make_buffer_rsrc((void*)p.v, 0, 2u * 8 * SK * DH * 2, 0x00020000);
  const unsigned kvoff = (unsigned)(((n16 & 3) * 4 + (n16 >> 2)) * 256 + g4 * 16);
  const unsigned vvoff = (unsigned)lane * 16;
  const unsigned sbase = (unsigned)((bat * 8 + kvh) * SK + split * 2048 + ((streamer ? 4 * DEPTH : 0) + (wave & 3)) * 16) * (DH * 2);
  constexpr unsigned TSTEP = 4 * 16 * DH * 2;
  u32x4 kb[DEPTH][4], vb[DEPTH][4];
#define ISSUE(d_, so_) do { \
    _Pragma("unroll") for (int s_ = 0; s_ < 4; ++s_) kb[d_][s_] = __builtin_amdgcn_raw_buffer_load_b128(kr, kvoff + s_ * 64, (so_), AUX_K); \
    _Pragma("unroll") for (int s_ = 0; s_ < 4; ++s_) vb[d_][s_] = __builtin_amdgcn_raw_buffer_load_b128(vr, vvoff + s_ * 1024, (so_), AUX_V); \
    __builtin_amdgcn_sched_barrier(0); } while (0)
  u64 tag;
  if (__builtin_expect(streamer, 1)) {
    STAMP(9);
#pragma unroll
    for (int d = 0; d < PRE_BAR; ++d) ISSUE(d, sbase + d * TSTEP);
    WG_BARRIER();
#pragma unroll
    for (int d = PRE_BAR; d < DEPTH; ++d) ISSUE(d, sbase + d * TSTEP);
    tag = LOAD_EPOCH() + 1;
  } else {
    const u32x4 xa = *reinterpret_cast<const u32x4*>(p.x + 8 * lane);
    const u32x4 xb = *reinterpret_cast<const u32x4*>(p.x + DM + 8 * lane);
    tag = LOAD_EPOCH() + 1;
    __builtin_amdgcn_sched_barrier(0);
    const int qrow = (w >> 6) * 128 + wave * 32 + frow;
    const unsigned short* wqp = p.wq + (size_t)qrow * NQ + (w & 63) * 64 + fcc * 8;
    u32x4 wv[4];
#pragma unroll
    for (int k = 0; k < 4; ++k) wv[k] = LDNT(wqp + (size_t)k * 8 * NQ);
    if (tid < 16) flag_w[tid] = 0u;
    WG_BARRIER();
    layernorm_to_lds(xa, xb, xn_s, lane);
    const unsigned t40_lo = (unsigned)(tag << 24), t40_hi = (unsigned)(tag >> 8);
    const bool b3 = (lane >> 3) & 1;
    {
      float qa[16];
#pragma unroll
      for (int j = 0; j < 16; ++j) qa[j] = 0.f;
#pragma unroll
      for (int k = 0; k < 4; ++k) {
        const f32x2 xv = *reinterpret_cast<const f32x2*>(xn_s + (qrow + 8 * k) * 2);
#pragma unroll
        for (int j = 0; j < 4; ++j) {
          const float l0 = bf_lo(wv[k][j]), h0 = bf_hi(wv[k][j]);
          qa[2 * j] = fmaf(xv[0], l0, qa[2 * j]); qa[2 * j + 1] = fmaf(xv[0], h0, qa[2 * j + 1]);
          qa[8 + 2 * j] = fmaf(xv[1], l0, qa[8 + 2 * j]); qa[8 + 2 * j + 1] = fmaf(xv[1], h0, qa[8 + 2 * j + 1]);
        }
      }
#pragma unroll
      for (int j = 0; j < 8; ++j) qa[j] = tr32(qa[j], qa[j + 8]);
#pragma unroll
      for (int j = 0; j < 4; ++j) qa[j] = tr16(qa[j], qa[j + 4]);
      *reinterpret_cast<f32x2*>(red_q + wave * 128 + (lane >> 5) * 64 + fcc * 8 + ((lane >> 3) & 3) * 2) = f32x2{tr8(qa[0], qa[2], b3), tr8(qa[1], qa[3], b3)};
      SET_FLAG(wave);
    }
    u32x4 w1v[8];
    const bool has_f1 = w < NFF;
    const int f1row = (w / 28) * 64 + frow;
    if (wave == 0) {
      while (!__all(GET_FLAG(lane & 3))) __builtin_amdgcn_s_sleep(0);
      f32x2 t = *reinterpret_cast<const f32x2*>(red_q + 2 * lane);
#pragma unroll
      for (int v = 1; v < 4; ++v) t += *reinterpret_cast<const f32x2*>(red_q + v * 128 + 2 * lane);
      STG(gr24x2(t[0], t[1], t40_lo, t40_hi), WS_QG + (unsigned)(((w >> 6) * 2 + (lane >> 5)) * NQ + (w & 63) * 64 + (lane & 31) * 2) * 8);
      STAMP(1);
    } else if (wave == 1 && has_f1) {
#pragma unroll
      for (int k = 0; k < 8; ++k) w1v[k] = LDNT(p.w1 + (size_t)(f1row + 8 * k) * DFF + (w % 28) * 64 + fcc * 8);
    }
    __builtin_amdgcn_sched_barrier(0);
    {
      const unsigned qoff = WS_QG + (unsigned)(bat * NQ + kvh * 512 + wave * 128 + 2 * lane) * 8;
      u32x4 qg[4];
      unsigned spins = 0;
      __builtin_amdgcn_s_sleep(Q_DELAY);
#pragma unroll
      for (int k = 0; k < 4; ++k) qg[k] = LDG(qoff + k * (2 * NQ * 8));
      for (;;) {
        const bool k0 = __all(ok24x2(qg[0], t40_lo, t40_hi)), k1 = __all(ok24x2(qg[1], t40_lo, t40_hi));
        const bool k2 = __all(ok24x2(qg[2], t40_lo, t40_hi)), k3 = __all(ok24x2(qg[3], t40_lo, t40_hi));
        if ((k0 & k1 & k2 & k3) || ++spins >= SPIN_CAP) break;
        __builtin_amdgcn_s_sleep(8);
        asm volatile("" ::: "memory");
        if (!k0) qg[0] = LDG(qoff);
        if (!k1) qg[1] = LDG(qoff + 1 * (2 * NQ * 8));
        if (!k2) qg[2] = LDG(qoff + 2 * (2 * NQ * 8));
        if (!k3) qg[3] = LDG(qoff + 3 * (2 * NQ * 8));
      }
      q_s[wave * 64 + lane] = cvtpk((val24(qg[0][0]) + val24(qg[1][0])) + (val24(qg[2][0]) + val24(qg[3][0])),
                                    (val24(qg[0][2]) + val24(qg[1][2])) + (val24(qg[2][2]) + val24(qg[3][2])));
      SET_FLAG(4 + wave);
      STAMP(11);
    }
    if (wave == 1 && has_f1) {
      float ha[16];
#pragma unroll
      for (int j = 0; j < 16; ++j) ha[j] = 0.f;
#pragma unroll
      for (int k = 0; k < 8; ++k) {
        const f32x2 xv = *reinterpret_cast<const f32x2*>(xn_s + (f1row + 8 * k) * 2);
#pragma unroll
        for (int j = 0; j < 4; ++j) {
          const float l0 = bf_lo(w1v[k][j]), h0 = bf_hi(w1v[k][j]);
          ha[2 * j] = fmaf(xv[0], l0, ha[2 * j]); ha[2 * j + 1] = fmaf(xv[0], h0, ha[2 * j + 1]);
          ha[8 + 2 * j] = fmaf(xv[1], l0, ha[8 + 2 * j]); ha[8 + 2 * j + 1] = fmaf(xv[1], h0, ha[8 + 2 * j + 1]);
        }
      }
#pragma unroll
      for (int j = 0; j < 8; ++j) ha[j] = tr32(ha[j], ha[j + 8]);
#pragma unroll
      for (int j = 0; j < 4; ++j) ha[j] = tr16(ha[j], ha[j + 4]);
      { const int u = fcc * 8 + (frow & 3) * 2;
        tr_h[u * 2 + (lane >> 5)] = tr8(ha[0], ha[2], b3); tr_h[u * 2 + 2 + (lane >> 5)] = tr8(ha[1], ha[3], b3); }
      const f32x2 t = *reinterpret_cast<const f32x2*>(tr_h + 2 * lane);
      STG(gr24x2(t[0], t[1], t40_lo, t40_hi), WS_HP + (unsigned)(w * 128 + 2 * lane) * 8);
      STAMP(10);
    } else if (wave == 2) {
      u32x4 t[16];
#pragma unroll
      for (int k = 0; k < 16; ++k) t[k] = LDNT(p.wo + (size_t)(kvh * 512 + (idx >> 2) * 64 + k * 4 + g4) * DM + (idx & 3) * 128 + n16 * 8);
#pragma unroll
      for (int k = 0; k < 16; ++k) wo_s[(k * 4 + g4) * 17 + n16] = t[k];
    }
    __builtin_amdgcn_sched_barrier(0);
#pragma unroll
    for (int d = 0; d < DEPTH; ++d) ISSUE(d, sbase + d * TSTEP);
  }

  {
    const unsigned msk0 = (n16 & 3) == 0 ? 0x0000ffffu : (n16 & 3) == 1 ? 0xffff0000u : 0u;
    const unsigned msk1 = (n16 & 3) == 2 ? 0x0000ffffu : (n16 & 3) == 3 ? 0xffff0000u : 0u;
    float m_run = -INFINITY, l_run = 0.f;
    f32x4 olo = {0.f, 0.f, 0.f, 0.f}, ohi = {0.f, 0.f, 0.f, 0.f};
    while (!__all(GET_FLAG(4 + (lane & 3)))) __builtin_amdgcn_s_sleep(1);
    bf16x8 qf[4];
#pragma unroll
    for (int s = 0; s < 4; ++s) qf[s] = *reinterpret_cast<const bf16x8*>(q_s + ((n16 >> 2) * DH + s * 32 + g4 * 8) / 2);
    STAMP(2);
#define TILE(d) do { \
      f32x4 sc = {0.f, 0.f, 0.f, 0.f}; \
      _Pragma("unroll") for (int s = 0; s < 4; ++s) sc = __builtin_amdgcn_mfma_f32_16x16x32_bf16(__builtin_bit_cast(bf16x8, kb[d][s]), qf[s], sc, 0, 0, 0); \
      float tmax = fmaxf(fmaxf(sc[0], sc[1]), fmaxf(sc[2], sc[3])); \
      tmax = xrow_max(tmax); \
      const float mnew = fmaxf(m_run, tmax); \
      const float alpha = __builtin_amdgcn_exp2f((m_run - mnew) * SOFT_C); \
      const float mc = -mnew * SOFT_C; \
      float pr[4]; \
      _Pragma("unroll") for (int r = 0; r < 4; ++r) pr[r] = __builtin_amdgcn_exp2f(fmaf(sc[r], SOFT_C, mc)); \
      l_run = fmaf(l_run, alpha, (pr[0] + pr[1]) + (pr[2] + pr[3])); \
      m_run = mnew; \
      olo *= alpha; ohi *= alpha; \
      _Pragma("unroll") for (int r = 0; r < 4; ++r) { \
        const unsigned pk = cvtpk(pr[r], pr[r]); \
        const u32x2 bp = {pk & msk0, pk & msk1}; \
        const u32x2 vlo = {vb[d][r][0], vb[d][r][1]}, vhi = {vb[d][r][2], vb[d][r][3]}; \
        olo = __builtin_amdgcn_mfma_f32_16x16x16bf16_1k(__builtin_bit_cast(s16x4, vlo), __builtin_bit_cast(s16x4, bp), olo, 0, 0, 0); \
        ohi = __builtin_amdgcn_mfma_f32_16x16x16bf16_1k(__builtin_bit_cast(s16x4, vhi), __builtin_bit_cast(s16x4, bp), ohi, 0, 0, 0); \
      } \
      __builtin_amdgcn_sched_barrier(0); } while (0)
    if (streamer) {
#pragma unroll
      for (int d = 0; d < REM; ++d) { TILE(d); ISSUE(d, sbase + (unsigned)(DEPTH + d) * TSTEP); }
#pragma unroll 1
      for (int it = 0; it < NFULL - 1; ++it) {
        const unsigned snext = sbase + (unsigned)(REM + (it + 1) * DEPTH) * TSTEP;
#pragma unroll
        for (int d = 0; d < DEPTH; ++d) {
          TILE((d + REM) % DEPTH);
          ISSUE((d + REM) % DEPTH, snext + d * TSTEP);
        }
      }
#pragma unroll
      for (int d = 0; d < DEPTH; ++d) TILE((d + REM) % DEPTH);
    } else {
#pragma unroll
      for (int d = 0; d < DEPTH; ++d) TILE(d);
    }
#undef TILE
#undef ISSUE
    STAMP(3);
    l_run = xrow_sum(l_run);
    float* mo = mrg_o + wave * 512 + (n16 >> 2) * DH + g4 * 32 + (n16 & 3);
#pragma unroll
    for (int r = 0; r < 4; ++r) { mo[r * 8] = olo[r]; mo[r * 8 + 4] = ohi[r]; }
    if (g4 == 0 && (n16 & 3) == 0) { mrg_m[wave * 4 + (n16 >> 2)] = m_run; mrg_l[wave * 4 + (n16 >> 2)] = l_run; }
  }
  const unsigned t40_lo = (unsigned)(tag << 24), t40_hi = (unsigned)(tag >> 8);
  if (wave == 3) {
    const bool b3 = (lane >> 3) & 1;
    if (w < NFF) {
      u32x4 w2v[8];
#pragma unroll
      for (int k = 0; k < 8; ++k) w2v[k] = LDNT(p.w2 + (size_t)((w >> 3) * 64 + frow + 8 * k) * DM + (w & 7) * 64 + fcc * 8);
      const unsigned hoff = WS_HP + (unsigned)((w >> 3) * 128 + 2 * lane) * 8;
      u32x4 hg[8];
      unsigned spins = 0;
      for (;;) {
        bool ok = true;
#pragma unroll
        for (int k = 0; k < 8; ++k) hg[k] = LDG(hoff + k * (28 * 128 * 8));
#pragma unroll
        for (int k = 0; k < 8; ++k) ok &= ok24x2(hg[k], t40_lo, t40_hi);
        if (__all(ok) || ++spins >= SPIN_CAP) break;
        __builtin_amdgcn_s_sleep(32);
        asm volatile("" ::: "memory");
      }
      float h0 = 0.f, h1 = 0.f;
#pragma unroll
      for (int k = 0; k < 8; ++k) { h0 += val24(hg[k][0]); h1 += val24(hg[k][2]); }
      *reinterpret_cast<f32x2*>(h_s + 2 * lane) = f32x2{fmaxf(rbf(h0), 0.f), fmaxf(rbf(h1), 0.f)};
      float ya[16];
#pragma unroll
      for (int j = 0; j < 16; ++j) ya[j] = 0.f;
#pragma unroll
      for (int k = 0; k < 8; ++k) {
        const f32x2 hv = *reinterpret_cast<const f32x2*>(h_s + (frow + 8 * k) * 2);
#pragma unroll
        for (int j = 0; j < 4; ++j) {
          const float l0 = bf_lo(w2v[k][j]), hh = bf_hi(w2v[k][j]);
          ya[2 * j] = fmaf(hv[0], l0, ya[2 * j]); ya[2 * j + 1] = fmaf(hv[0], hh, ya[2 * j + 1]);
          ya[8 + 2 * j] = fmaf(hv[1], l0, ya[8 + 2 * j]); ya[8 + 2 * j + 1] = fmaf(hv[1], hh, ya[8 + 2 * j + 1]);
        }
      }
#pragma unroll
      for (int j = 0; j < 8; ++j) ya[j] = tr32(ya[j], ya[j + 8]);
#pragma unroll
      for (int j = 0; j < 4; ++j) ya[j] = tr16(ya[j], ya[j + 4]);
      asm volatile("" ::: "memory");
      *reinterpret_cast<f32x2*>(h_s + ((fcc >> 1) * 2 + (lane >> 5)) * 16 + ((frow & 3) * 2 + (fcc & 1)) * 2) = f32x2{tr8(ya[0], ya[2], b3), tr8(ya[1], ya[3], b3)};
      const f32x2 t = *reinterpret_cast<const f32x2*>(h_s + 2 * lane);
      STG(gr24x2(t[0], t[1], t40_lo, t40_hi), WS_YP + (unsigned)((w >> 3) * 1024 + (w & 7) * 128 + 2 * lane) * 8);
    }
    STAMP(8);
    if (w < 64) {
      const unsigned yoff = WS_YP + (unsigned)(frow * 1024 + w * 16 + fcc * 2) * 8;
      u32x4 yg[4];
      unsigned spins = 0;
      for (;;) {
        __builtin_amdgcn_s_sleep(64);
        bool ok = true;
#pragma unroll
        for (int k = 0; k < 4; ++k) yg[k] = (k < 3 || lane < 32) ? LDG(yoff + k * 8 * 8192) : u32x4{t40_lo, t40_hi, t40_lo, t40_hi};
#pragma unroll
        for (int k = 0; k < 4; ++k) ok &= ok24x2(yg[k], t40_lo, t40_hi);
        if (__all(ok) || ++spins >= SPIN_CAP) break;
        asm volatile("" ::: "memory");
      }
      float y0 = (val24(yg[0][0]) + val24(yg[1][0])) + (val24(yg[2][0]) + val24(yg[3][0]));
      float y1 = (val24(yg[0][2]) + val24(yg[1][2])) + (val24(yg[2][2]) + val24(yg[3][2]));
      y0 += dpp<ROR8>(y0); y0 = rbf(xrow_sum(y0));
      y1 += dpp<ROR8>(y1); y1 = rbf(xrow_sum(y1));
      if (lane < 8) *reinterpret_cast<f32x2*>(g_s + lane * 2) = f32x2{rbf(y0 / (1.f + __expf(-y0))), rbf(y1 / (1.f + __expf(-y1)))};
    }
    STAMP(12);
  }
  asm volatile("" :: "v"(t40_lo), "v"(t40_hi));
  __syncthreads();
  {
    const int h = tid >> 7;
    float mw[8], M = -INFINITY;
#pragma unroll
    for (int v = 0; v < 8; ++v) { mw[v] = mrg_m[v * 4 + h]; M = fmaxf(M, mw[v]); }
    float o = 0.f, L = 0.f;
#pragma unroll
    for (int v = 0; v < 8; ++v) {
      const float f = __builtin_amdgcn_exp2f((mw[v] - M) * SOFT_C);
      o = fmaf(f, mrg_o[v * 512 + tid], o); L = fmaf(f, mrg_l[v * 4 + h], L);
    }
    __builtin_amdgcn_raw_buffer_store_b64(gr24(f24(o), t40_lo, t40_hi), wsr, WS_AP + (unsigned)(w * 512 + tid) * 8, 0, AUX_SC1);
    if ((tid & 127) < 2) {
      const unsigned mb = __float_as_uint(M);
      const unsigned p0 = (tid & 1) ? f24(L) : (mb >> 8), p1 = (tid & 1) ? 0u : (mb & 0xffu);
      STG((u32x4{p0 | t40_lo, t40_hi, p1 | t40_lo, t40_hi}), WS_ST + (unsigned)(((kvh * 4 + h) * 32 + bat * 16 + split) * 4 + 2 * (tid & 1)) * 8);
    }
  }
  STAMP(4);
  float wof[16];
  {
    const u32x4 woa = wo_s[(lane >> 1) * 17 + wave * 2 + (lane & 1)], wob = wo_s[(32 + (lane >> 1)) * 17 + wave * 2 + (lane & 1)];
#pragma unroll
    for (int j = 0; j < 4; ++j) { wof[2 * j] = bf_lo(woa[j]); wof[2 * j + 1] = bf_hi(woa[j]); wof[8 + 2 * j] = bf_lo(wob[j]); wof[8 + 2 * j + 1] = bf_hi(wob[j]); }
  }
  {
    if (wave == 0) {
      const unsigned o2 = WS_ST + (unsigned)((kvh * 4 + (idx >> 3)) * 128 + lane * 2) * 8;
      u32x4 gs;
      unsigned spins = 0;
      for (;;) {
        gs = LDG(o2);
        const int missing = __popcll(__ballot(!ok24x2(gs, t40_lo, t40_hi)));
        if (missing <= 2 * EARLY_GO) SET_FLAG(8);
        if (missing == 0 || ++spins >= SPIN_CAP) break;
        asm volatile("" ::: "memory");
      }
      SET_FLAG(8);
      const float mine = __uint_as_float((gs[0] << 8) | ((lane & 1) ? 0u : (gs[2] & 0xffu)));
      const float other = dpp<XOR1>(mine);
      const float m = (lane & 1) ? other : mine, l = (lane & 1) ? mine : other;
      float M = m; M = fmaxf(M, dpp<XOR2>(M)); M = fmaxf(M, dpp<XOR7>(M)); M = fmaxf(M, dpp<ROR8>(M));
      { auto t = __builtin_amdgcn_permlane16_swap(__float_as_uint(M), __float_as_uint(M), false, false); M = fmaxf(__uint_as_float(t[0]), __uint_as_float(t[1])); }
      const float f = __builtin_amdgcn_exp2f((m - M) * SOFT_C);
      float L = f * l; L += dpp<XOR2>(L); L += dpp<XOR7>(L); L += dpp<ROR8>(L); L = tr16(L, L);
      if ((lane & 1) == 0) c_scl[lane >> 1] = f / L;
    } else {
      while (!GET_FLAG(8)) __builtin_amdgcn_s_sleep(0);
    }
    const int pp0 = wave * 4 + (lane >> 5), pp1 = pp0 + 2;
    const unsigned o1 = WS_AP + (unsigned)((kvh + 8 * (pp0 >> 4) + 16 * (pp0 & 15)) * 512 + (idx >> 2) * 64 + (lane & 31) * 2) * 8;
    u32x4 ga, gb;
    unsigned spins = 0;
    for (;;) {
      ga = LDG(o1); gb = LDG(o1 + 2 * 16 * 512 * 8);
      if (__all(ok24x2(ga, t40_lo, t40_hi) & ok24x2(gb, t40_lo, t40_hi)) || ++spins >= SPIN_CAP) break;
      asm volatile("" ::: "memory");
    }
    *reinterpret_cast<f32x2*>(c_acc + pp0 * 64 + (lane & 31) * 2) = f32x2{val24(ga[0]), val24(ga[2])};
    *reinterpret_cast<f32x2*>(c_acc + pp1 * 64 + (lane & 31) * 2) = f32x2{val24(gb[0]), val24(gb[2])};
  }
  __syncthreads();
  STAMP(5);
  {
    float o0 = 0.f, o1 = 0.f;
#pragma unroll
    for (int s4 = 0; s4 < 4; ++s4) {
      const f32x4 sa = *reinterpret_cast<const f32x4*>(c_scl + s4 * 4), sb = *reinterpret_cast<const f32x4*>(c_scl + 16 + s4 * 4);
#pragma unroll
      for (int j = 0; j < 4; ++j) { o0 = fmaf(sa[j], c_acc[(s4 * 4 + j) * 64 + lane], o0); o1 = fmaf(sb[j], c_acc[(16 + s4 * 4 + j) * 64 + lane], o1); }
    }
    o0 = rbf(o0); o1 = rbf(o1);
    const float o00 = __shfl(o0, lane >> 1), o01 = __shfl(o0, 32 + (lane >> 1)), o10 = __shfl(o1, lane >> 1), o11 = __shfl(o1, 32 + (lane >> 1));
    float sa[8];
#pragma unroll
    for (int j = 0; j < 8; ++j) sa[j] = tr32(fmaf(o01, wof[8 + j], o00 * wof[j]), fmaf(o11, wof[8 + j], o10 * wof[j]));
#pragma unroll
    for (int j = 0; j < 4; ++j) sa[j] = tr16(sa[j], sa[j + 4]);
    const bool b1 = (lane >> 1) & 1;
    float r0 = (b1 ? sa[2] : sa[0]) + dpp<XOR2>(b1 ? sa[0] : sa[2]), r1 = (b1 ? sa[3] : sa[1]) + dpp<XOR2>(b1 ? sa[1] : sa[3]);
    r0 += dpp<ROR4>(r0); r0 += dpp<ROR8>(r0);
    r1 += dpp<ROR4>(r1); r1 += dpp<ROR8>(r1);
    if ((lane & 12) == 0)
      STG(gr24x2(r0, r1, t40_lo, t40_hi),
          WS_SP + (unsigned)((kvh * 8 + (idx >> 2)) * 1024 + (((idx & 3) * 8 + wave) * 2 + (lane >> 5)) * 16 + (((lane >> 4) & 1) * 4 + (lane & 3)) * 2) * 8);
  }
  STAMP(6);
  if (w < 64) {
    const unsigned soff = WS_SP + (unsigned)((wave * 8 + frow) * 1024 + w * 16 + fcc * 2) * 8;
    const int oidx = (w & 1) * DM + (w >> 1) * 16 + (fcc & 1) * 8 + (fcc >> 2) * 4 + ((fcc >> 1) & 1) * 2;
    const unsigned xr = *reinterpret_cast<const unsigned*>(p.x + oidx);
    u32x4 sg;
    unsigned spins = 0;
    for (;;) {
      sg = LDG(soff);
      if (__all(ok24x2(sg, t40_lo, t40_hi)) || ++spins >= SPIN_CAP) break;
      asm volatile("" ::: "memory");
    }
    float s0 = val24(sg[0]), s1 = val24(sg[2]);
    s0 += dpp<ROR8>(s0); s0 = xrow_sum(s0);
    s1 += dpp<ROR8>(s1); s1 = xrow_sum(s1);
    if (lane < 8) *reinterpret_cast<f32x2*>(fin_s + wave * 16 + lane * 2) = f32x2{s0, s1};
    __syncthreads();
    if (wave == 0 && lane < 8) {
      float sa = 0.f, sb = 0.f;
#pragma unroll
      for (int v = 0; v < 8; ++v) { const f32x2 t = *reinterpret_cast<const f32x2*>(fin_s + v * 16 + lane * 2); sa += t[0]; sb += t[1]; }
      const f32x2 g = *reinterpret_cast<const f32x2*>(g_s + lane * 2);
      const float r0 = bf_lo(xr) + rbf(rbf(sa) * g[0]), r1 = bf_hi(xr) + rbf(rbf(sb) * g[1]);
      *reinterpret_cast<unsigned*>(p.out + oidx) = cvtpk(r0, r1);
      if (w == 0 && lane == 0) __hip_atomic_store(reinterpret_cast<u64*>(p.ws + WS_EPOCH), tag, __ATOMIC_RELAXED, __HIP_MEMORY_SCOPE_AGENT);
    }
  }
  STAMP(7);
}

template <int STRIDE>
__global__ void __launch_bounds__(256) touch_k(const unsigned* __restrict__ K, const unsigned* __restrict__ V, unsigned* out) {
  const int w = blockIdx.x;
  const size_t base = ((size_t)(w % 16) * 32768 + (w / 16) * 2048) * 64;
  unsigned acc = 0;
  for (unsigned o = threadIdx.x * (STRIDE / 4); o < 2048u * 64; o += 256 * (STRIDE / 4)) acc ^= __builtin_nontemporal_load(K + base + o) ^ __builtin_nontemporal_load(V + base + o);
  if (acc == 0x12345678u) out[threadIdx.x] = acc;
}
}

extern "C" void kernel_launch(void* const* d_in, const int* in_sizes, int n_in, void* d_out, int out_size, void* d_ws, size_t ws_size,
                              hipStream_t stream) {
  Params p;
  p.x = (const unsigned short*)d_in[0];
  p.k = (const unsigned short*)d_in[1];
  p.v = (const unsigned short*)d_in[2];
  p.wq = (const unsigned short*)d_in[3];
  p.wo = (const unsigned short*)d_in[4];
  p.w1 = (const unsigned short*)d_in[6];
  p.w2 = (const unsigned short*)d_in[7];
  p.out = (unsigned short*)d_out;
  p.ws = (unsigned char*)d_ws;
  hipLaunchKernelGGL(fused_decode_kernel, dim3(NWG), dim3(NTHR), 0, stream, p);
}
```

```cpp
#include <hip/hip_runtime.h>
#include <hip/hip_bf16.h>
#include <stdint.h>

typedef unsigned u32x4 __attribute__((ext_vector_type(4)));
typedef unsigned u32x2 __attribute__((ext_vector_type(2)));
typedef float f32x4 __attribute__((ext_vector_type(4)));
typedef float f32x2 __attribute__((ext_vector_type(2)));
typedef __bf16 bf16x2 __attribute__((ext_vector_type(2)));
typedef __bf16 bf16x8 __attribute__((ext_vector_type(8)));
typedef short s16x4 __attribute__((ext_vector_type(4)));
typedef unsigned long long u64;

namespace {

constexpr int NWG = 256, NTHR = 512, NWAVE = 8;
constexpr int SK = 32768, DH = 128, DM = 512, DFF = 1792, NQ = 4096;
constexpr int DEPTH = 6;
constexpr int NFULL = 4, REM = 2;
static_assert(4 * (NFULL * DEPTH + REM) + 4 * DEPTH == 128, "128 tiles of 16 keys per workgroup");
constexpr int NFF = DFF / 8;
constexpr unsigned WS_EPOCH = 0;
constexpr unsigned WS_QG = 4096;
constexpr unsigned WS_HP = WS_QG + 4 * 2 * NQ * 8;
constexpr unsigned WS_AP = WS_HP + 8 * 28 * 128 * 8;
constexpr unsigned WS_ST = WS_AP + NWG * 4 * 128 * 8;
constexpr unsigned WS_YP = WS_ST + 8 * 4 * 32 * 4 * 8;
constexpr unsigned WS_SP = WS_YP + 28 * 1024 * 8;
constexpr unsigned WS_END = WS_SP + 64 * 1024 * 8;
constexpr unsigned WS_DBG = 8u << 20;
constexpr unsigned SPIN_CAP = 1u << 16;
constexpr int AUX_NT = 2, AUX_SC1 = 16;
constexpr float SOFT_C = 0.08838834764831845f * 1.4426950408889634f;

__device__ __forceinline__ float bf_lo(unsigned u) { return __uint_as_float(u << 16); }
__device__ __forceinline__ float bf_hi(unsigned u) { return __uint_as_float(u & 0xffff0000u); }
__device__ __forceinline__ unsigned cvtpk(float lo, float hi) {
  return __builtin_bit_cast(unsigned, __builtin_convertvector(f32x2{lo, hi}, bf16x2));
}
__device__ __forceinline__ float rbf(float x) { return bf_lo(cvtpk(x, x)); }
template <int CTRL> __device__ __forceinline__ float dpp(float x) {
  return __builtin_bit_cast(float, __builtin_amdgcn_mov_dpp(__builtin_bit_cast(int, x), CTRL, 0xf, 0xf, true));
}
constexpr int ROR1 = 0x121, ROR2 = 0x122, ROR4 = 0x124, ROR8 = 0x128, XOR1 = 0xB1, XOR2 = 0x4E, XOR7 = 0x141;
__device__ __forceinline__ float xrow_max(float x) {
  auto s = __builtin_amdgcn_permlane16_swap(__float_as_uint(x), __float_as_uint(x), false, false);
  x = fmaxf(__uint_as_float(s[0]), __uint_as_float(s[1]));
  auto t = __builtin_amdgcn_permlane32_swap(__float_as_uint(x), __float_as_uint(x), false, false);
  return fmaxf(__uint_as_float(t[0]), __uint_as_float(t[1]));
}
__device__ __forceinline__ float xrow_sum(float x) {
  auto s = __builtin_amdgcn_permlane16_swap(__float_as_uint(x), __float_as_uint(x), false, false);
  x = __uint_as_float(s[0]) + __uint_as_float(s[1]);
  auto t = __builtin_amdgcn_permlane32_swap(__float_as_uint(x), __float_as_uint(x), false, false);
  return __uint_as_float(t[0]) + __uint_as_float(t[1]);
}
__device__ __forceinline__ float wave_sum(float x) {
  x += dpp<ROR8>(x); x += dpp<ROR4>(x); x += dpp<ROR2>(x); x += dpp<ROR1>(x);
  return xrow_sum(x);
}
__device__ __forceinline__ float tr32(float a, float b) {
  auto t = __builtin_amdgcn_permlane32_swap(__float_as_uint(a), __float_as_uint(b), false, false);
  return __uint_as_float(t[0]) + __uint_as_float(t[1]);
}
__device__ __forceinline__ float tr16(float a, float b) {
  auto t = __builtin_amdgcn_permlane16_swap(__float_as_uint(a), __float_as_uint(b), false, false);
  return __uint_as_float(t[0]) + __uint_as_float(t[1]);
}
__device__ __forceinline__ float tr8(float a, float b, bool bit3) {
  const float keep = bit3 ? b : a, send = bit3 ? a : b;
  return keep + dpp<ROR8>(send);
}
__device__ __forceinline__ float tr4(float a, float b, bool bit2) {
  const float keep = bit2 ? b : a, send = bit2 ? a : b;
  return keep + dpp<XOR7>(send);
}
__device__ __forceinline__ unsigned f24(float x) { return (__float_as_uint(x) + 0x80u) >> 8; }
__device__ __forceinline__ u32x2 gr24(unsigned payload24, unsigned tlo8, unsigned thi) { return u32x2{payload24 | tlo8, thi}; }
__device__ __forceinline__ bool ok24(unsigned lo, unsigned hi, unsigned tlo8, unsigned thi) { return ((lo & 0xff000000u) == tlo8) & (hi == thi); }
__device__ __forceinline__ float val24(unsigned lo) { return __uint_as_float(lo << 8); }

struct Params {
  const unsigned short* x; const unsigned short* k; const unsigned short* v; const unsigned short* wq; const unsigned short* wo;
  const unsigned short* w1; const unsigned short* w2; unsigned short* out; unsigned char* ws;
};

#define START_WIN 2
#define WARM_LIVE0 0
#define STAMP(i) do {} while (0)
#define LDW1(p_) LDNT(p_)
#define Q_DELAY 32
#define EARLY_GO 3
#define YP_DELAY 1
#define AUX_K 2
#define AUX_V 2
#define PRE_BAR 2
#define LDNT(p_) __builtin_nontemporal_load(reinterpret_cast<const u32x4*>(p_))

__device__ __forceinline__ void layernorm_to_lds(const u32x4 xa, const u32x4 xb, float* dst, int lane) {
  float e0[8], e1[8];
#pragma unroll
  for (int j = 0; j < 4; ++j) { e0[2 * j] = bf_lo(xa[j]); e0[2 * j + 1] = bf_hi(xa[j]); e1[2 * j] = bf_lo(xb[j]); e1[2 * j + 1] = bf_hi(xb[j]); }
  float s0 = 0.f, s1 = 0.f;
#pragma unroll
  for (int j = 0; j < 8; ++j) { s0 += e0[j]; s1 += e1[j]; }
  s0 = wave_sum(s0) * (1.f / DM); s1 = wave_sum(s1) * (1.f / DM);
  float v0 = 0.f, v1 = 0.f;
#pragma unroll
  for (int j = 0; j < 8; ++j) { const float a = e0[j] - s0, b = e1[j] - s1; v0 = fmaf(a, a, v0); v1 = fmaf(b, b, v1); }
  v0 = wave_sum(v0) * (1.f / DM); v1 = wave_sum(v1) * (1.f / DM);
  const float m0 = rbf(s0), m1 = rbf(s1);
  const float d0 = 1.f / rbf(sqrtf(rbf(rbf(v0) + 1e-5f))), d1 = 1.f / rbf(sqrtf(rbf(rbf(v1) + 1e-5f)));
#pragma unroll
  for (int j = 0; j < 8; j += 2)
    *reinterpret_cast<f32x4*>(dst + (8 * lane + j) * 2) =
        f32x4{rbf(rbf(e0[j] - m0) * d0), rbf(rbf(e1[j] - m1) * d1), rbf(rbf(e0[j + 1] - m0) * d0), rbf(rbf(e1[j + 1] - m1) * d1)};
}

__device__ __forceinline__ bool ok24x2(const u32x4 g, unsigned tlo8, unsigned thi) { return ok24(g[0], g[1], tlo8, thi) & ok24(g[2], g[3], tlo8, thi); }
__device__ __forceinline__ u32x4 gr24x2(float a, float b, unsigned tlo8, unsigned thi) { return u32x4{f24(a) | tlo8, thi, f24(b) | tlo8, thi}; }

__global__ void __launch_bounds__(NTHR, 2) fused_decode_kernel(Params p) {
  __shared__ __attribute__((aligned(16))) float lds[13312];
  float* xn_s = lds;
  float* red_q = lds + 1024;
  float* mrg_m = lds + 1536;
  float* mrg_l = lds + 1568;
  float* c_scl = lds + 1600;
  float* fin_s = lds + 1632;
  float* tr_h = lds + 1760;
  float* h_s = lds + 1888;
  float* g_s = lds + 2016;
  unsigned* q_s = reinterpret_cast<unsigned*>(lds + 2048);
  unsigned* flag_w = reinterpret_cast<unsigned*>(lds + 2304);
  float* c_acc = lds + 2560;
  float* mrg_o = lds + 4608;
  u32x4* wo_s = reinterpret_cast<u32x4*>(lds + 8704);

  const int w = blockIdx.x, tid = threadIdx.x, lane = tid & 63;
  const int wave = __builtin_amdgcn_readfirstlane(tid >> 6);
  const int kvh = w & 7, bat = (w >> 3) & 1, split = w >> 4, idx = w >> 3;
  const int n16 = lane & 15, g4 = lane >> 4, frow = lane >> 3, fcc = lane & 7;
  STAMP(0);

  const __amdgpu_buffer_rsrc_t wsr = __builtin_amdgcn_make_buffer_rsrc((void*)p.ws, 0, WS_END, 0x00020000);
#define LOAD_EPOCH() __hip_atomic_load(reinterpret_cast<const u64*>(p.ws + WS_EPOCH), __ATOMIC_RELAXED, __HIP_MEMORY_SCOPE_AGENT)
#define WG_BARRIER() do { __builtin_amdgcn_sched_barrier(0); asm volatile("s_waitcnt lgkmcnt(0)\n\ts_barrier" ::: "memory"); __builtin_amdgcn_sched_barrier(0); } while (0)
#define LDG(off_) __builtin_amdgcn_raw_buffer_load_b128(wsr, (off_), 0, AUX_SC1)
#define STG(v_, off_) __builtin_amdgcn_raw_buffer_store_b128((v_), wsr, (off_), 0, AUX_SC1)
  const bool streamer = wave >= 4;

#define SET_FLAG(i_) __hip_atomic_store(&flag_w[i_], 1u, __ATOMIC_RELEASE, __HIP_MEMORY_SCOPE_WORKGROUP)
#define GET_FLAG(i_) (__hip_atomic_load(&flag_w[i_], __ATOMIC_ACQUIRE, __HIP_MEMORY_SCOPE_WORKGROUP) != 0u)
  const __amdgpu_buffer_rsrc_t kr = __builtin_amdgcn_make_buffer_rsrc((void*)p.k, 0, 2u * 8 * SK * DH * 2, 0x00020000);
  const __amdgpu_buffer_rsrc_t vr = __builtin_amdgcn_make_buffer_rsrc((void*)p.v, 0, 2u * 8 * SK * DH * 2, 0x00020000);
  const unsigned kvoff = (unsigned)(((n16 & 3) * 4 + (n16 >> 2)) * 256 + g4 * 16);
  const unsigned vvoff = (unsigned)lane * 16;
  const unsigned sbase = (unsigned)((bat * 8 + kvh) * SK + split * 2048 + ((streamer ? 4 * DEPTH : 0) + (wave & 3)) * 16) * (DH * 2);
  constexpr unsigned TSTEP = 4 * 16 * DH * 2;
  u32x4 kb[DEPTH][4], vb[DEPTH][4];
#define ISSUE(d_, so_) do { \
    _Pragma("unroll") for (int s_ = 0; s_ < 4; ++s_) kb[d_][s_] = __builtin_amdgcn_raw_buffer_load_b128(kr, kvoff + s_ * 64, (so_), AUX_K); \
    _Pragma("unroll") for (int s_ = 0; s_ < 4; ++s_) vb[d_][s_] = __builtin_amdgcn_raw_buffer_load_b128(vr, vvoff + s_ * 1024, (so_), AUX_V); \
    __builtin_amdgcn_sched_barrier(0); } while (0)
  u64 tag;
  if (__builtin_expect(streamer, 1)) {
    STAMP(9);
#pragma unroll
    for (int d = 0; d < PRE_BAR; ++d) ISSUE(d, sbase + d * TSTEP);
    WG_BARRIER();
#pragma unroll
    for (int d = PRE_BAR; d < DEPTH; ++d) ISSUE(d, sbase + d * TSTEP);
    tag = LOAD_EPOCH() + 1;
  } else {
    const u32x4 xa = *reinterpret_cast<const u32x4*>(p.x + 8 * lane);
    const u32x4 xb = *reinterpret_cast<const u32x4*>(p.x + DM + 8 * lane);
    tag = LOAD_EPOCH() + 1;
    __builtin_amdgcn_sched_barrier(0);
    const int qrow = (w >> 6) * 128 + wave * 32 + frow;
    const unsigned short* wqp = p.wq + (size_t)qrow * NQ + (w & 63) * 64 + fcc * 8;
    u32x4 wv[4];
#pragma unroll
    for (int k = 0; k < 4; ++k) wv[k] = LDNT(wqp + (size_t)k * 8 * NQ);
    if (tid < 16) flag_w[tid] = 0u;
    WG_BARRIER();
    layernorm_to_lds(xa, xb, xn_s, lane);
    const unsigned t40_lo = (unsigned)(tag << 24), t40_hi = (unsigned)(tag >> 8);
    const bool b3 = (lane >> 3) & 1;
    {
      float qa[16];
#pragma unroll
      for (int j = 0; j < 16; ++j) qa[j] = 0.f;
#pragma unroll
      for (int k = 0; k < 4; ++k) {
        const f32x2 xv = *reinterpret_cast<const f32x2*>(xn_s + (qrow + 8 * k) * 2);
#pragma unroll
        for (int j = 0; j < 4; ++j) {
          const float l0 = bf_lo(wv[k][j]), h0 = bf_hi(wv[k][j]);
          qa[2 * j] = fmaf(xv[0], l0, qa[2 * j]); qa[2 * j + 1] = fmaf(xv[0], h0, qa[2 * j + 1]);
          qa[8 + 2 * j] = fmaf(xv[1], l0, qa[8 + 2 * j]); qa[8 + 2 * j + 1] = fmaf(xv[1], h0, qa[8 + 2 * j + 1]);
        }
      }
#pragma unroll
      for (int j = 0; j < 8; ++j) qa[j] = tr32(qa[j], qa[j + 8]);
#pragma unroll
      for (int j = 0; j < 4; ++j) qa[j] = tr16(qa[j], qa[j + 4]);
      *reinterpret_cast<f32x2*>(red_q + wave * 128 + (lane >> 5) * 64 + fcc * 8 + ((lane >> 3) & 3) * 2) = f32x2{tr8(qa[0], qa[2], b3), tr8(qa[1], qa[3], b3)};
      SET_FLAG(wave);
    }
    u32x4 w1v[8];
    const bool has_f1 = w < NFF;
    const int f1row = (w / 28) * 64 + frow;
    if (wave == 0) {
      while (!__all(GET_FLAG(lane & 3))) __builtin_amdgcn_s_sleep(0);
      f32x2 t = *reinterpret_cast<const f32x2*>(red_q + 2 * lane);
#pragma unroll
      for (int v = 1; v < 4; ++v) t += *reinterpret_cast<const f32x2*>(red_q + v * 128 + 2 * lane);
      STG(gr24x2(t[0], t[1], t40_lo, t40_hi), WS_QG + (unsigned)(((w >> 6) * 2 + (lane >> 5)) * NQ + (w & 63) * 64 + (lane & 31) * 2) * 8);
      STAMP(1);
    } else if (wave == 1 && has_f1) {
#pragma unroll
      for (int k = 0; k < 8; ++k) w1v[k] = LDNT(p.w1 + (size_t)(f1row + 8 * k) * DFF + (w % 28) * 64 + fcc * 8);
    }
    __builtin_amdgcn_sched_barrier(0);
    {
      const unsigned qoff = WS_QG + (unsigned)(bat * NQ + kvh * 512 + wave * 128 + 2 * lane) * 8;
      u32x4 qg[4];
      unsigned spins = 0;
      __builtin_amdgcn_s_sleep(Q_DELAY);
#pragma unroll
      for (int k = 0; k < 4; ++k) qg[k] = LDG(qoff + k * (2 * NQ * 8));
      for (;;) {
        const bool k0 = __all(ok24x2(qg[0], t40_lo, t40_hi)), k1 = __all(ok24x2(qg[1], t40_lo, t40_hi));
        const bool k2 = __all(ok24x2(qg[2], t40_lo, t40_hi)), k3 = __all(ok24x2(qg[3], t40_lo, t40_hi));
        if ((k0 & k1 & k2 & k3) || ++spins >= SPIN_CAP) break;
        __builtin_amdgcn_s_sleep(8);
        asm volatile("" ::: "memory");
        if (!k0) qg[0] = LDG(qoff);
        if (!k1) qg[1] = LDG(qoff + 1 * (2 * NQ * 8));
        if (!k2) qg[2] = LDG(qoff + 2 * (2 * NQ * 8));
        if (!k3) qg[3] = LDG(qoff + 3 * (2 * NQ * 8));
      }
      q_s[wave * 64 + lane] = cvtpk((val24(qg[0][0]) + val24(qg[1][0])) + (val24(qg[2][0]) + val24(qg[3][0])),
                                    (val24(qg[0][2]) + val24(qg[1][2])) + (val24(qg[2][2]) + val24(qg[3][2])));
      SET_FLAG(4 + wave);
      STAMP(11);
    }
    if (wave == 1 && has_f1) {
      float ha[16];
#pragma unroll
      for (int j = 0; j < 16; ++j) ha[j] = 0.f;
#pragma unroll
      for (int k = 0; k < 8; ++k) {
        const f32x2 xv = *reinterpret_cast<const f32x2*>(xn_s + (f1row + 8 * k) * 2);
#pragma unroll
        for (int j = 0; j < 4; ++j) {
          const float l0 = bf_lo(w1v[k][j]), h0 = bf_hi(w1v[k][j]);
          ha[2 * j] = fmaf(xv[0], l0, ha[2 * j]); ha[2 * j + 1] = fmaf(xv[0], h0, ha[2 * j + 1]);
          ha[8 + 2 * j] = fmaf(xv[1], l0, ha[8 + 2 * j]); ha[8 + 2 * j + 1] = fmaf(xv[1], h0, ha[8 + 2 * j + 1]);
        }
      }
#pragma unroll
      for (int j = 0; j < 8; ++j) ha[j] = tr32(ha[j], ha[j + 8]);
#pragma unroll
      for (int j = 0; j < 4; ++j) ha[j] = tr16(ha[j], ha[j + 4]);
      { const int u = fcc * 8 + (frow & 3) * 2;
        tr_h[u * 2 + (lane >> 5)] = tr8(ha[0], ha[2], b3); tr_h[u * 2 + 2 + (lane >> 5)] = tr8(ha[1], ha[3], b3); }
      const f32x2 t = *reinterpret_cast<const f32x2*>(tr_h + 2 * lane);
      STG(gr24x2(t[0], t[1], t40_lo, t40_hi), WS_HP + (unsigned)(w * 128 + 2 * lane) * 8);
      STAMP(10);
    } else if (wave == 2) {
      u32x4 t[16];
#pragma unroll
      for (int k = 0; k < 16; ++k) t[k] = LDNT(p.wo + (size_t)(kvh * 512 + (idx >> 2) * 64 + k * 4 + g4) * DM + (idx & 3) * 128 + n16 * 8);
#pragma unroll
      for (int k = 0; k < 16; ++k) wo_s[(k * 4 + g4) * 17 + n16] = t[k];
    }
    __builtin_amdgcn_sched_barrier(0);
#pragma unroll
    for (int d = 0; d < DEPTH; ++d) ISSUE(d, sbase + d * TSTEP);
  }

  {
    const unsigned msk0 = (n16 & 3) == 0 ? 0x0000ffffu : (n16 & 3) == 1 ? 0xffff0000u : 0u;
    const unsigned msk1 = (n16 & 3) == 2 ? 0x0000ffffu : (n16 & 3) == 3 ? 0xffff0000u : 0u;
    float m_run = -INFINITY, l_run = 0.f;
    f32x4 olo = {0.f, 0.f, 0.f, 0.f}, ohi = {0.f, 0.f, 0.f, 0.f};
    while (!__all(GET_FLAG(4 + (lane & 3)))) __builtin_amdgcn_s_sleep(1);
    bf16x8 qf[4];
#pragma unroll
    for (int s = 0; s < 4; ++s) qf[s] = *reinterpret_cast<const bf16x8*>(q_s + ((n16 >> 2) * DH + s * 32 + g4 * 8) / 2);
    STAMP(2);
#define TILE(d) do { \
      f32x4 sc = {0.f, 0.f, 0.f, 0.f}; \
      _Pragma("unroll") for (int s = 0; s < 4; ++s) sc = __builtin_amdgcn_mfma_f32_16x16x32_bf16(__builtin_bit_cast(bf16x8, kb[d][s]), qf[s], sc, 0, 0, 0); \
      float tmax = fmaxf(fmaxf(sc[0], sc[1]), fmaxf(sc[2], sc[3])); \
      tmax = xrow_max(tmax); \
      const float mnew = fmaxf(m_run, tmax); \
      const float alpha = __builtin_amdgcn_exp2f((m_run - mnew) * SOFT_C); \
      const float mc = -mnew * SOFT_C; \
      float pr[4]; \
      _Pragma("unroll") for (int r = 0; r < 4; ++r) pr[r] = __builtin_amdgcn_exp2f(fmaf(sc[r], SOFT_C, mc)); \
      l_run = fmaf(l_run, alpha, (pr[0] + pr[1]) + (pr[2] + pr[3])); \
      m_run = mnew; \
      olo *= alpha; ohi *= alpha; \
      _Pragma("unroll") for (int r = 0; r < 4; ++r) { \
        const unsigned pk = cvtpk(pr[r], pr[r]); \
        const u32x2 bp = {pk & msk0, pk & msk1}; \
        const u32x2 vlo = {vb[d][r][0], vb[d][r][1]}, vhi = {vb[d][r][2], vb[d][r][3]}; \
        olo = __builtin_amdgcn_mfma_f32_16x16x16bf16_1k(__builtin_bit_cast(s16x4, vlo), __builtin_bit_cast(s16x4, bp), olo, 0, 0, 0); \
        ohi = __builtin_amdgcn_mfma_f32_16x16x16bf16_1k(__builtin_bit_cast(s16x4, vhi), __builtin_bit_cast(s16x4, bp), ohi, 0, 0, 0); \
      } \
      __builtin_amdgcn_sched_barrier(0); } while (0)
    if (streamer) {
#pragma unroll
      for (int d = 0; d < REM; ++d) { TILE(d); ISSUE(d, sbase + (unsigned)(DEPTH + d) * TSTEP); }
#pragma unroll 1
      for (int it = 0; it < NFULL - 1; ++it) {
        const unsigned snext = sbase + (unsigned)(REM + (it + 1) * DEPTH) * TSTEP;
#pragma unroll
        for (int d = 0; d < DEPTH; ++d) {
          TILE((d + REM) % DEPTH);
          ISSUE((d + REM) % DEPTH, snext + d * TSTEP);
        }
      }
#pragma unroll
      for (int d = 0; d < DEPTH; ++d) TILE((d + REM) % DEPTH);
    } else {
#pragma unroll
      for (int d = 0; d < DEPTH; ++d) TILE(d);
    }
#undef TILE
#undef ISSUE
    STAMP(3);
    l_run = xrow_sum(l_run);
    float* mo = mrg_o + wave * 512 + (n16 >> 2) * DH + g4 * 32 + (n16 & 3);
#pragma unroll
    for (int r = 0; r < 4; ++r) { mo[r * 8] = olo[r]; mo[r * 8 + 4] = ohi[r]; }
    if (g4 == 0 && (n16 & 3) == 0) { mrg_m[wave * 4 + (n16 >> 2)] = m_run; mrg_l[wave * 4 + (n16 >> 2)] = l_run; }
  }
  const unsigned t40_lo = (unsigned)(tag << 24), t40_hi = (unsigned)(tag >> 8);
  if (wave == 3) {
    const bool b3 = (lane >> 3) & 1;
    if (w < NFF) {
      u32x4 w2v[8];
#pragma unroll
      for (int k = 0; k < 8; ++k) w2v[k] = LDNT(p.w2 + (size_t)((w >> 3) * 64 + frow + 8 * k) * DM + (w & 7) * 64 + fcc * 8);
      const unsigned hoff = WS_HP + (unsigned)((w >> 3) * 128 + 2 * lane) * 8;
      u32x4 hg[8];
      unsigned spins = 0;
      for (;;) {
        bool ok = true;
#pragma unroll
        for (int k = 0; k < 8; ++k) hg[k] = LDG(hoff + k * (28 * 128 * 8));
#pragma unroll
        for (int k = 0; k < 8; ++k) ok &= ok24x2(hg[k], t40_lo, t40_hi);
        if (__all(ok) || ++spins >= SPIN_CAP) break;
        __builtin_amdgcn_s_sleep(32);
        asm volatile("" ::: "memory");
      }
      float h0 = 0.f, h1 = 0.f;
#pragma unroll
      for (int k = 0; k < 8; ++k) { h0 += val24(hg[k][0]); h1 += val24(hg[k][2]); }
      *reinterpret_cast<f32x2*>(h_s + 2 * lane) = f32x2{fmaxf(rbf(h0), 0.f), fmaxf(rbf(h1), 0.f)};
      float ya[16];
#pragma unroll
      for (int j = 0; j < 16; ++j) ya[j] = 0.f;
#pragma unroll
      for (int k = 0; k < 8; ++k) {
        const f32x2 hv = *reinterpret_cast<const f32x2*>(h_s + (frow + 8 * k) * 2);
#pragma unroll
        for (int j = 0; j < 4; ++j) {
          const float l0 = bf_lo(w2v[k][j]), hh = bf_hi(w2v[k][j]);
          ya[2 * j] = fmaf(hv[0], l0, ya[2 * j]); ya[2 * j + 1] = fmaf(hv[0], hh, ya[2 * j + 1]);
          ya[8 + 2 * j] = fmaf(hv[1], l0, ya[8 + 2 * j]); ya[8 + 2 * j + 1] = fmaf(hv[1], hh, ya[8 + 2 * j + 1]);
        }
      }
#pragma unroll
      for (int j = 0; j < 8; ++j) ya[j] = tr32(ya[j], ya[j + 8]);
#pragma unroll
      for (int j = 0; j < 4; ++j) ya[j] = tr16(ya[j], ya[j + 4]);
      asm volatile("" ::: "memory");
      *reinterpret_cast<f32x2*>(h_s + ((fcc >> 1) * 2 + (lane >> 5)) * 16 + ((frow & 3) * 2 + (fcc & 1)) * 2) = f32x2{tr8(ya[0], ya[2], b3), tr8(ya[1], ya[3], b3)};
      const f32x2 t = *reinterpret_cast<const f32x2*>(h_s + 2 * lane);
      STG(gr24x2(t[0], t[1], t40_lo, t40_hi), WS_YP + (unsigned)((w >> 3) * 1024 + (w & 7) * 128 + 2 * lane) * 8);
    }
    STAMP(8);
    if (w < 64) {
      const unsigned yoff = WS_YP + (unsigned)(frow * 1024 + w * 16 + fcc * 2) * 8;
      u32x4 yg[4];
      unsigned spins = 0;
      for (;;) {
        __builtin_amdgcn_s_sleep(64);
        bool ok = true;
#pragma unroll
        for (int k = 0; k < 4; ++k) yg[k] = (k < 3 || lane < 32) ? LDG(yoff + k * 8 * 8192) : u32x4{t40_lo, t40_hi, t40_lo, t40_hi};
#pragma unroll
        for (int k = 0; k < 4; ++k) ok &= ok24x2(yg[k], t40_lo, t40_hi);
        if (__all(ok) || ++spins >= SPIN_CAP) break;
        asm volatile("" ::: "memory");
      }
      float y0 = (val24(yg[0][0]) + val24(yg[1][0])) + (val24(yg[2][0]) + val24(yg[3][0]));
      float y1 = (val24(yg[0][2]) + val24(yg[1][2])) + (val24(yg[2][2]) + val24(yg[3][2]));
      y0 += dpp<ROR8>(y0); y0 = rbf(xrow_sum(y0));
      y1 += dpp<ROR8>(y1); y1 = rbf(xrow_sum(y1));
      if (lane < 8) *reinterpret_cast<f32x2*>(g_s + lane * 2) = f32x2{rbf(y0 / (1.f + __expf(-y0))), rbf(y1 / (1.f + __expf(-y1)))};
    }
    STAMP(12);
  }
  asm volatile("" :: "v"(t40_lo), "v"(t40_hi));
  __syncthreads();
  {
    const int h = tid >> 7;
    float mw[8], M = -INFINITY;
#pragma unroll
    for (int v = 0; v < 8; ++v) { mw[v] = mrg_m[v * 4 + h]; M = fmaxf(M, mw[v]); }
    float o = 0.f, L = 0.f;
#pragma unroll
    for (int v = 0; v < 8; ++v) {
      const float f = __builtin_amdgcn_exp2f((mw[v] - M) * SOFT_C);
      o = fmaf(f, mrg_o[v * 512 + tid], o); L = fmaf(f, mrg_l[v * 4 + h], L);
    }
    __builtin_amdgcn_raw_buffer_store_b64(gr24(f24(o), t40_lo, t40_hi), wsr, WS_AP + (unsigned)(w * 512 + tid) * 8, 0, AUX_SC1);
    if ((tid & 127) < 2) {
      const unsigned mb = __float_as_uint(M);
      const unsigned p0 = (tid & 1) ? f24(L) : (mb >> 8), p1 = (tid & 1) ? 0u : (mb & 0xffu);
      STG((u32x4{p0 | t40_lo, t40_hi, p1 | t40_lo, t40_hi}), WS_ST + (unsigned)(((kvh * 4 + h) * 32 + bat * 16 + split) * 4 + 2 * (tid & 1)) * 8);
    }
  }
  STAMP(4);
  float wof[16];
  {
    const u32x4 woa = wo_s[(lane >> 1) * 17 + wave * 2 + (lane & 1)], wob = wo_s[(32 + (lane >> 1)) * 17 + wave * 2 + (lane & 1)];
#pragma unroll
    for (int j = 0; j < 4; ++j) { wof[2 * j] = bf_lo(woa[j]); wof[2 * j + 1] = bf_hi(woa[j]); wof[8 + 2 * j] = bf_lo(wob[j]); wof[8 + 2 * j + 1] = bf_hi(wob[j]); }
  }
  {
    if (wave == 0) {
      const unsigned o2 = WS_ST + (unsigned)((kvh * 4 + (idx >> 3)) * 128 + lane * 2) * 8;
      u32x4 gs;
      unsigned spins = 0;
      for (;;) {
        gs = LDG(o2);
        const int missing = __popcll(__ballot(!ok24x2(gs, t40_lo, t40_hi)));
        if (missing <= 2 * EARLY_GO) SET_FLAG(8);
        if (missing == 0 || ++spins >= SPIN_CAP) break;
        asm volatile("" ::: "memory");
      }
      SET_FLAG(8);
      const float mine = __uint_as_float((gs[0] << 8) | ((lane & 1) ? 0u : (gs[2] & 0xffu)));
      const float other = dpp<XOR1>(mine);
      const float m = (lane & 1) ? other : mine, l = (lane & 1) ? mine : other;
      float M = m; M = fmaxf(M, dpp<XOR2>(M)); M = fmaxf(M, dpp<XOR7>(M)); M = fmaxf(M, dpp<ROR8>(M));
      { auto t = __builtin_amdgcn_permlane16_swap(__float_as_uint(M), __float_as_uint(M), false, false); M = fmaxf(__uint_as_float(t[0]), __uint_as_float(t[1])); }
      const float f = __builtin_amdgcn_exp2f((m - M) * SOFT_C);
      float L = f * l; L += dpp<XOR2>(L); L += dpp<XOR7>(L); L += dpp<ROR8>(L); L = tr16(L, L);
      if ((lane & 1) == 0) c_scl[lane >> 1] = f / L;
    } else {
      while (!GET_FLAG(8)) __builtin_amdgcn_s_sleep(0);
    }
    const int pp0 = wave * 4 + (lane >> 5), pp1 = pp0 + 2;
    const unsigned o1 = WS_AP + (unsigned)((kvh + 8 * (pp0 >> 4) + 16 * (pp0 & 15)) * 512 + (idx >> 2) * 64 + (lane & 31) * 2) * 8;
    u32x4 ga, gb;
    unsigned spins = 0;
    for (;;) {
      ga = LDG(o1); gb = LDG(o1 + 2 * 16 * 512 * 8);
      if (__all(ok24x2(ga, t40_lo, t40_hi) & ok24x2(gb, t40_lo, t40_hi)) || ++spins >= SPIN_CAP) break;
      asm volatile("" ::: "memory");
    }
    *reinterpret_cast<f32x2*>(c_acc + pp0 * 64 + (lane & 31) * 2) = f32x2{val24(ga[0]), val24(ga[2])};
    *reinterpret_cast<f32x2*>(c_acc + pp1 * 64 + (lane & 31) * 2) = f32x2{val24(gb[0]), val24(gb[2])};
  }
  __syncthreads();
  STAMP(5);
  {
    float o0 = 0.f, o1 = 0.f;
#pragma unroll
    for (int s4 = 0; s4 < 4; ++s4) {
      const f32x4 sa = *reinterpret_cast<const f32x4*>(c_scl + s4 * 4), sb = *reinterpret_cast<const f32x4*>(c_scl + 16 + s4 * 4);
#pragma unroll
      for (int j = 0; j < 4; ++j) { o0 = fmaf(sa[j], c_acc[(s4 * 4 + j) * 64 + lane], o0); o1 = fmaf(sb[j], c_acc[(16 + s4 * 4 + j) * 64 + lane], o1); }
    }
    o0 = rbf(o0); o1 = rbf(o1);
    const float o00 = __shfl(o0, lane >> 1), o01 = __shfl(o0, 32 + (lane >> 1)), o10 = __shfl(o1, lane >> 1), o11 = __shfl(o1, 32 + (lane >> 1));
    float sa[8];
#pragma unroll
    for (int j = 0; j < 8; ++j) sa[j] = tr32(fmaf(o01, wof[8 + j], o00 * wof[j]), fmaf(o11, wof[8 + j], o10 * wof[j]));
#pragma unroll
    for (int j = 0; j < 4; ++j) sa[j] = tr16(sa[j], sa[j + 4]);
    const bool b1 = (lane >> 1) & 1;
    float r0 = (b1 ? sa[2] : sa[0]) + dpp<XOR2>(b1 ? sa[0] : sa[2]), r1 = (b1 ? sa[3] : sa[1]) + dpp<XOR2>(b1 ? sa[1] : sa[3]);
    r0 += dpp<ROR4>(r0); r0 += dpp<ROR8>(r0);
    r1 += dpp<ROR4>(r1); r1 += dpp<ROR8>(r1);
    if ((lane & 12) == 0)
      STG(gr24x2(r0, r1, t40_lo, t40_hi),
          WS_SP + (unsigned)((kvh * 8 + (idx >> 2)) * 1024 + (((idx & 3) * 8 + wave) * 2 + (lane >> 5)) * 16 + (((lane >> 4) & 1) * 4 + (lane & 3)) * 2) * 8);
  }
  STAMP(6);
  if (w < 64) {
    const unsigned soff = WS_SP + (unsigned)((wave * 8 + frow) * 1024 + w * 16 + fcc * 2) * 8;
    const int oidx = (w & 1) * DM + (w >> 1) * 16 + (fcc & 1) * 8 + (fcc >> 2) * 4 + ((fcc >> 1) & 1) * 2;
    const unsigned xr = *reinterpret_cast<const unsigned*>(p.x + oidx);
    u32x4 sg;
    unsigned spins = 0;
    for (;;) {
      sg = LDG(soff);
      if (__all(ok24x2(sg, t40_lo, t40_hi)) || ++spins >= SPIN_CAP) break;
      asm volatile("" ::: "memory");
    }
    float s0 = val24(sg[0]), s1 = val24(sg[2]);
    s0 += dpp<ROR8>(s0); s0 = xrow_sum(s0);
    s1 += dpp<ROR8>(s1); s1 = xrow_sum(s1);
    if (lane < 8) *reinterpret_cast<f32x2*>(fin_s + wave * 16 + lane * 2) = f32x2{s0, s1};
    __syncthreads();
    if (wave == 0 && lane < 8) {
      float sa = 0.f, sb = 0.f;
#pragma unroll
      for (int v = 0; v < 8; ++v) { const f32x2 t = *reinterpret_cast<const f32x2*>(fin_s + v * 16 + lane * 2); sa += t[0]; sb += t[1]; }
      const f32x2 g = *reinterpret_cast<const f32x2*>(g_s + lane * 2);
      const float r0 = bf_lo(xr) + rbf(rbf(sa) * g[0]), r1 = bf_hi(xr) + rbf(rbf(sb) * g[1]);
      *reinterpret_cast<unsigned*>(p.out + oidx) = cvtpk(r0, r1);
      if (w == 0 && lane == 0) __hip_atomic_store(reinterpret_cast<u64*>(p.ws + WS_EPOCH), tag, __ATOMIC_RELAXED, __HIP_MEMORY_SCOPE_AGENT);
    }
  }
  STAMP(7);
}

__global__ void __launch_bounds__(64) wait_k(int ticks) {
  const unsigned long long t0 = __builtin_amdgcn_s_memrealtime();
  while (__builtin_amdgcn_s_memrealtime() - t0 < (unsigned long long)ticks) __builtin_amdgcn_s_sleep(8);
}
template <int WHICH>
__global__ void __launch_bounds__(256) half_k(const u32x4* __restrict__ K, unsigned* out) {
  const size_t base = (size_t)blockIdx.x * 32768 + threadIdx.x;
  u32x4 acc = {0, 0, 0, 0};
#pragma unroll 8
  for (int i = 0; i < 128; ++i) acc ^= __builtin_nontemporal_load(K + base + i * 256);
  unsigned r = acc.x ^ acc.y ^ acc.z ^ acc.w;
  if (r == 0x12345678u) out[threadIdx.x] = r;
}
}

extern "C" void kernel_launch(void* const* d_in, const int* in_sizes, int n_in, void* d_out, int out_size, void* d_ws, size_t ws_size,
                              hipStream_t stream) {
  Params p;
  p.x = (const unsigned short*)d_in[0];
  p.k = (const unsigned short*)d_in[1];
  p.v = (const unsigned short*)d_in[2];
  p.wq = (const unsigned short*)d_in[3];
  p.wo = (const unsigned short*)d_in[4];
  p.w1 = (const unsigned short*)d_in[6];
  p.w2 = (const unsigned short*)d_in[7];
  p.out = (unsigned short*)d_out;
  p.ws = (unsigned char*)d_ws;
  hipLaunchKernelGGL(half_k<0>, dim3(256), dim3(256), 0, stream, (const u32x4*)d_in[1], (unsigned*)((char*)d_ws + (64u << 20)));
  hipLaunchKernelGGL(fused_decode_kernel, dim3(NWG), dim3(NTHR), 0, stream, p);
}
```

```cpp
#include <hip/hip_runtime.h>
#include <hip/hip_bf16.h>
#include <stdint.h>

typedef unsigned u32x4 __attribute__((ext_vector_type(4)));
typedef unsigned u32x2 __attribute__((ext_vector_type(2)));
typedef float f32x4 __attribute__((ext_vector_type(4)));
typedef float f32x2 __attribute__((ext_vector_type(2)));
typedef __bf16 bf16x2 __attribute__((ext_vector_type(2)));
typedef __bf16 bf16x8 __attribute__((ext_vector_type(8)));
typedef short s16x4 __attribute__((ext_vector_type(4)));
typedef unsigned long long u64;

namespace {

constexpr int NWG = 256, NTHR = 512, NWAVE = 8;
constexpr int SK = 32768, DH = 128, DM = 512, DFF = 1792, NQ = 4096;
#define DEPTH_ 6
constexpr int DEPTH = DEPTH_;
#define HT DEPTH
constexpr int NFULL = (32 - HT) / DEPTH, REM = (32 - HT) % DEPTH;
static_assert(4 * (NFULL * DEPTH + REM) + 4 * HT == 128, "128 tiles of 16 keys per workgroup");
constexpr int NFF = DFF / 8;
constexpr unsigned WS_EPOCH = 0;
constexpr unsigned WS_QG = 4096;
constexpr unsigned WS_HP = WS_QG + 4 * 2 * NQ * 8;
constexpr unsigned WS_AP = WS_HP + 8 * 28 * 128 * 8;
constexpr unsigned WS_ST = WS_AP + NWG * 4 * 128 * 8;
constexpr unsigned WS_YP = WS_ST + 8 * 4 * 32 * 4 * 8;
constexpr unsigned WS_SP = WS_YP + 28 * 1024 * 8;
constexpr unsigned WS_END = WS_SP + 64 * 1024 * 8;
constexpr unsigned WS_DBG = 8u << 20;
constexpr unsigned SPIN_CAP = 1u << 16;
constexpr int AUX_NT = 2, AUX_SC1 = 16;
constexpr float SOFT_C = 0.08838834764831845f * 1.4426950408889634f;

__device__ __forceinline__ float bf_lo(unsigned u) { return __uint_as_float(u << 16); }
__device__ __forceinline__ float bf_hi(unsigned u) { return __uint_as_float(u & 0xffff0000u); }
__device__ __forceinline__ unsigned cvtpk(float lo, float hi) {
  return __builtin_bit_cast(unsigned, __builtin_convertvector(f32x2{lo, hi}, bf16x2));
}
__device__ __forceinline__ float rbf(float x) { return bf_lo(cvtpk(x, x)); }
template <int CTRL> __device__ __forceinline__ float dpp(float x) {
  return __builtin_bit_cast(float, __builtin_amdgcn_mov_dpp(__builtin_bit_cast(int, x), CTRL, 0xf, 0xf, true));
}
constexpr int ROR1 = 0x121, ROR2 = 0x122, ROR4 = 0x124, ROR8 = 0x128, XOR1 = 0xB1, XOR2 = 0x4E, XOR7 = 0x141;
__device__ __forceinline__ float xrow_max(float x) {
  auto s = __builtin_amdgcn_permlane16_swap(__float_as_uint(x), __float_as_uint(x), false, false);
  x = fmaxf(__uint_as_float(s[0]), __uint_as_float(s[1]));
  auto t = __builtin_amdgcn_permlane32_swap(__float_as_uint(x), __float_as_uint(x), false, false);
  return fmaxf(__uint_as_float(t[0]), __uint_as_float(t[1]));
}
__device__ __forceinline__ float xrow_sum(float x) {
  auto s = __builtin_amdgcn_permlane16_swap(__float_as_uint(x), __float_as_uint(x), false, false);
  x = __uint_as_float(s[0]) + __uint_as_float(s[1]);
  auto t = __builtin_amdgcn_permlane32_swap(__float_as_uint(x), __float_as_uint(x), false, false);
  return __uint_as_float(t[0]) + __uint_as_float(t[1]);
}
__device__ __forceinline__ float wave_sum(float x) {
  x += dpp<ROR8>(x); x += dpp<ROR4>(x); x += dpp<ROR2>(x); x += dpp<ROR1>(x);
  return xrow_sum(x);
}
__device__ __forceinline__ float tr32(float a, float b) {
  auto t = __builtin_amdgcn_permlane32_swap(__float_as_uint(a), __float_as_uint(b), false, false);
  return __uint_as_float(t[0]) + __uint_as_float(t[1]);
}
__device__ __forceinline__ float tr16(float a, float b) {
  auto t = __builtin_amdgcn_permlane16_swap(__float_as_uint(a), __float_as_uint(b), false, false);
  return __uint_as_float(t[0]) + __uint_as_float(t[1]);
}
__device__ __forceinline__ float tr8(float a, float b, bool bit3) {
  const float keep = bit3 ? b : a, send = bit3 ? a : b;
  return keep + dpp<ROR8>(send);
}
__device__ __forceinline__ float tr4(float a, float b, bool bit2) {
  const float keep = bit2 ? b : a, send = bit2 ? a : b;
  return keep + dpp<XOR7>(send);
}
__device__ __forceinline__ unsigned f24(float x) { return (__float_as_uint(x) + 0x80u) >> 8; }
__device__ __forceinline__ u32x2 gr24(unsigned payload24, unsigned tlo8, unsigned thi) { return u32x2{payload24 | tlo8, thi}; }
__device__ __forceinline__ bool ok24(unsigned lo, unsigned hi, unsigned tlo8, unsigned thi) { return ((lo & 0xff000000u) == tlo8) & (hi == thi); }
__device__ __forceinline__ float val24(unsigned lo) { return __uint_as_float(lo << 8); }

struct Params {
  const unsigned short* x; const unsigned short* k; const unsigned short* v; const unsigned short* wq; const unsigned short* wo;
  const unsigned short* w1; const unsigned short* w2; unsigned short* out; unsigned char* ws; unsigned char* dbg;
};

#define START_WIN 2
#define WARM_LIVE0 0
#define STAMP(i) do {} while (0)
#define LDW1(p_) LDNT(p_)
#define Q_DELAY 32
#define EARLY_GO 3
#define YP_DELAY 1
#define AUX_K 2
#define AUX_V 2
#define PRE_BAR 2
#define LDNT(p_) __builtin_nontemporal_load(reinterpret_cast<const u32x4*>(p_))

__device__ __forceinline__ void layernorm_to_lds(const u32x4 xa, const u32x4 xb, float* dst, int lane) {
  float e0[8], e1[8];
#pragma unroll
  for (int j = 0; j < 4; ++j) { e0[2 * j] = bf_lo(xa[j]); e0[2 * j + 1] = bf_hi(xa[j]); e1[2 * j] = bf_lo(xb[j]); e1[2 * j + 1] = bf_hi(xb[j]); }
  float s0 = 0.f, s1 = 0.f;
#pragma unroll
  for (int j = 0; j < 8; ++j) { s0 += e0[j]; s1 += e1[j]; }
  s0 = wave_sum(s0) * (1.f / DM); s1 = wave_sum(s1) * (1.f / DM);
  float v0 = 0.f, v1 = 0.f;
#pragma unroll
  for (int j = 0; j < 8; ++j) { const float a = e0[j] - s0, b = e1[j] - s1; v0 = fmaf(a, a, v0); v1 = fmaf(b, b, v1); }
  v0 = wave_sum(v0) * (1.f / DM); v1 = wave_sum(v1) * (1.f / DM);
  const float m0 = rbf(s0), m1 = rbf(s1);
  const float d0 = 1.f / rbf(sqrtf(rbf(rbf(v0) + 1e-5f))), d1 = 1.f / rbf(sqrtf(rbf(rbf(v1) + 1e-5f)));
#pragma unroll
  for (int j = 0; j < 8; j += 2)
    *reinterpret_cast<f32x4*>(dst + (8 * lane + j) * 2) =
        f32x4{rbf(rbf(e0[j] - m0) * d0), rbf(rbf(e1[j] - m1) * d1), rbf(rbf(e0[j + 1] - m0) * d0), rbf(rbf(e1[j + 1] - m1) * d1)};
}

__device__ __forceinline__ bool ok24x2(const u32x4 g, unsigned tlo8, unsigned thi) { return ok24(g[0], g[1], tlo8, thi) & ok24(g[2], g[3], tlo8, thi); }
__device__ __forceinline__ u32x4 gr24x2(float a, float b, unsigned tlo8, unsigned thi) { return u32x4{f24(a) | tlo8, thi, f24(b) | tlo8, thi}; }

__global__ void __launch_bounds__(NTHR, 2) fused_decode_kernel(Params p) {
  __shared__ __attribute__((aligned(16))) float lds[13312];
  float* xn_s = lds;
  float* red_q = lds + 1024;
  float* mrg_m = lds + 1536;
  float* mrg_l = lds + 1568;
  float* c_scl = lds + 1600;
  float* fin_s = lds + 1632;
  float* tr_h = lds + 1760;
  float* h_s = lds + 1888;
  float* g_s = lds + 2016;
  unsigned* q_s = reinterpret_cast<unsigned*>(lds + 2048);
  unsigned* flag_w = reinterpret_cast<unsigned*>(lds + 2304);
  float* c_acc = lds + 2560;
  float* mrg_o = lds + 4608;
  u32x4* wo_s = reinterpret_cast<u32x4*>(lds + 8704);

  const int w = blockIdx.x, tid = threadIdx.x, lane = tid & 63;
  const int wave = __builtin_amdgcn_readfirstlane(tid >> 6);
  const int kvh = w & 7, bat = (w >> 3) & 1, split = w >> 4, idx = w >> 3;
  const int n16 = lane & 15, g4 = lane >> 4, frow = lane >> 3, fcc = lane & 7;
  STAMP(0);

  const __amdgpu_buffer_rsrc_t wsr = __builtin_amdgcn_make_buffer_rsrc((void*)p.ws, 0, WS_END, 0x00020000);
#define LOAD_EPOCH() __hip_atomic_load(reinterpret_cast<const u64*>(p.ws + WS_EPOCH), __ATOMIC_RELAXED, __HIP_MEMORY_SCOPE_AGENT)
#define WG_BARRIER() do { __builtin_amdgcn_sched_barrier(0); asm volatile("s_waitcnt lgkmcnt(0)\n\ts_barrier" ::: "memory"); __builtin_amdgcn_sched_barrier(0); } while (0)
#define LDG(off_) __builtin_amdgcn_raw_buffer_load_b128(wsr, (off_), 0, AUX_SC1)
#define LDC(off_) __builtin_amdgcn_raw_buffer_load_b128(wsr, (off_), 0, 0)
#define STG(v_, off_) __builtin_amdgcn_raw_buffer_store_b128((v_), wsr, (off_), 0, AUX_SC1)
#define FAKEG u32x4{t40_lo | 0x3c0000u, t40_hi, t40_lo | 0x3c0000u, t40_hi}
#define KEEPG(x_) ({ u32x4 t_ = (x_); asm volatile("" :: "v"(t_)); FAKEG; })
#define NOSTG(v_, off_) do { u32x4 t_ = (v_); asm volatile("" :: "v"(t_)); } while (0)
#define LDCQ LDC
#define STGQ STG
#define LDCF LDC
#define LDGF LDG
#define STGF STG
  const bool streamer = wave >= 4;
  const int f2kb = (w & 7) + 8 * (w >> 6), f2cb = (w >> 3) & 7;

#define SET_FLAG(i_) __hip_atomic_store(&flag_w[i_], 1u, __ATOMIC_RELEASE, __HIP_MEMORY_SCOPE_WORKGROUP)
#define GET_FLAG(i_) (__hip_atomic_load(&flag_w[i_], __ATOMIC_ACQUIRE, __HIP_MEMORY_SCOPE_WORKGROUP) != 0u)
  const __amdgpu_buffer_rsrc_t kr = __builtin_amdgcn_make_buffer_rsrc((void*)p.k, 0, 2u * 8 * SK * DH * 2, 0x00020000);
  const __amdgpu_buffer_rsrc_t vr = __builtin_amdgcn_make_buffer_rsrc((void*)p.v, 0, 2u * 8 * SK * DH * 2, 0x00020000);
  const unsigned kvoff = (unsigned)(((n16 & 3) * 4 + (n16 >> 2)) * 256 + g4 * 16);
  const unsigned vvoff = (unsigned)lane * 16;
  const unsigned sbase = (unsigned)((bat * 8 + kvh) * SK + split * 2048 + ((streamer ? 4 * HT : 0) + (wave & 3)) * 16) * (DH * 2);
  constexpr unsigned TSTEP = 4 * 16 * DH * 2;
  u32x4 kb[DEPTH][4], vb[DEPTH][4];
#define KDEF false
#define VDEF false
  const bool kdef = (KDEF), vdef = (VDEF);
#define ISSUE(d_, so_) do { \
    if (kdef) { _Pragma("unroll") for (int s_ = 0; s_ < 4; ++s_) kb[d_][s_] = __builtin_amdgcn_raw_buffer_load_b128(kr, kvoff + s_ * 64, (so_), 0); } \
    else { _Pragma("unroll") for (int s_ = 0; s_ < 4; ++s_) kb[d_][s_] = __builtin_amdgcn_raw_buffer_load_b128(kr, kvoff + s_ * 64, (so_), 2); } \
    if (vdef) { _Pragma("unroll") for (int s_ = 0; s_ < 4; ++s_) vb[d_][s_] = __builtin_amdgcn_raw_buffer_load_b128(vr, vvoff + s_ * 1024, (so_), 0); } \
    else { _Pragma("unroll") for (int s_ = 0; s_ < 4; ++s_) vb[d_][s_] = __builtin_amdgcn_raw_buffer_load_b128(vr, vvoff + s_ * 1024, (so_), 2); } \
    __builtin_amdgcn_sched_barrier(0); } while (0)
  u64 tag;
  if (__builtin_expect(streamer, 1)) {
    STAMP(9);
#pragma unroll
    for (int d = 0; d < PRE_BAR; ++d) ISSUE(d, sbase + d * TSTEP);
    WG_BARRIER();
#pragma unroll
    for (int d = PRE_BAR; d < DEPTH; ++d) ISSUE(d, sbase + d * TSTEP);
    tag = LOAD_EPOCH() + 1;
  } else {
    const u32x4 xa = *reinterpret_cast<const u32x4*>(p.x + 8 * lane);
    const u32x4 xb = *reinterpret_cast<const u32x4*>(p.x + DM + 8 * lane);
    tag = LOAD_EPOCH() + 1;
    __builtin_amdgcn_sched_barrier(0);
    const int qrow = (w >> 6) * 128 + wave * 32 + frow;
    const unsigned short* wqp = p.wq + (size_t)qrow * NQ + (w & 63) * 64 + fcc * 8;
    u32x4 wv[4];
#pragma unroll
    for (int k = 0; k < 4; ++k) wv[k] = LDNT(wqp + (size_t)k * 8 * NQ);
    __builtin_amdgcn_sched_barrier(0);
    const bool has_ff = w < NFF;
    const int f1row = (w / 28) * 64 + frow;
    u32x4 wt[16];
    if (wave == 1) {
      if (has_ff) {
#pragma unroll
        for (int k = 0; k < 8; ++k) wt[k] = LDNT(p.w1 + (size_t)(f1row + 8 * k) * DFF + (w % 28) * 64 + fcc * 8);
      }
    } else if (wave == 2) {
#pragma unroll
      for (int k = 0; k < 16; ++k) wt[k] = LDNT(p.wo + (size_t)(kvh * 512 + (idx >> 2) * 64 + k * 4 + g4) * DM + (idx & 3) * 128 + n16 * 8);
    } else if (wave == 3) {
      if (f2kb < 28) {
#pragma unroll
        for (int k = 0; k < 8; ++k) wt[k] = LDNT(p.w2 + (size_t)(f2kb * 64 + frow + 8 * k) * DM + f2cb * 64 + fcc * 8);
      }
    }
    if (tid < 16) flag_w[tid] = 0u;
    WG_BARRIER();
    layernorm_to_lds(xa, xb, xn_s, lane);
    const unsigned t40_lo = (unsigned)(tag << 24), t40_hi = (unsigned)(tag >> 8);
    const bool b3 = (lane >> 3) & 1;
    {
      float qa[16];
#pragma unroll
      for (int j = 0; j < 16; ++j) qa[j] = 0.f;
#pragma unroll
      for (int k = 0; k < 4; ++k) {
        const f32x2 xv = *reinterpret_cast<const f32x2*>(xn_s + (qrow + 8 * k) * 2);
#pragma unroll
        for (int j = 0; j < 4; ++j) {
          const float l0 = bf_lo(wv[k][j]), h0 = bf_hi(wv[k][j]);
          qa[2 * j] = fmaf(xv[0], l0, qa[2 * j]); qa[2 * j + 1] = fmaf(xv[0], h0, qa[2 * j + 1]);
          qa[8 + 2 * j] = fmaf(xv[1], l0, qa[8 + 2 * j]); qa[8 + 2 * j + 1] = fmaf(xv[1], h0, qa[8 + 2 * j + 1]);
        }
      }
#pragma unroll
      for (int j = 0; j < 8; ++j) qa[j] = tr32(qa[j], qa[j + 8]);
#pragma unroll
      for (int j = 0; j < 4; ++j) qa[j] = tr16(qa[j], qa[j + 4]);
      *reinterpret_cast<f32x2*>(red_q + wave * 128 + (lane >> 5) * 64 + fcc * 8 + ((lane >> 3) & 3) * 2) = f32x2{tr8(qa[0], qa[2], b3), tr8(qa[1], qa[3], b3)};
      SET_FLAG(wave);
    }
    if (wave == 0) {
      while (!__all(GET_FLAG(lane & 3))) __builtin_amdgcn_s_sleep(0);
      f32x2 t = *reinterpret_cast<const f32x2*>(red_q + 2 * lane);
#pragma unroll
      for (int v = 1; v < 4; ++v) t += *reinterpret_cast<const f32x2*>(red_q + v * 128 + 2 * lane);
      STGQ(gr24x2(t[0], t[1], t40_lo, t40_hi), WS_QG + (unsigned)(((w >> 6) * 2 + (lane >> 5)) * NQ + (w & 63) * 64 + (lane & 31) * 2) * 8);
      STAMP(1);
    } else if (wave == 1) {
      if (has_ff) {
        float ha[16];
#pragma unroll
        for (int j = 0; j < 16; ++j) ha[j] = 0.f;
#pragma unroll
        for (int k = 0; k < 8; ++k) {
          const f32x2 xv = *reinterpret_cast<const f32x2*>(xn_s + (f1row + 8 * k) * 2);
#pragma unroll
          for (int j = 0; j < 4; ++j) {
            const float l0 = bf_lo(wt[k][j]), h0 = bf_hi(wt[k][j]);
            ha[2 * j] = fmaf(xv[0], l0, ha[2 * j]); ha[2 * j + 1] = fmaf(xv[0], h0, ha[2 * j + 1]);
            ha[8 + 2 * j] = fmaf(xv[1], l0, ha[8 + 2 * j]); ha[8 + 2 * j + 1] = fmaf(xv[1], h0, ha[8 + 2 * j + 1]);
          }
        }
#pragma unroll
        for (int j = 0; j < 8; ++j) ha[j] = tr32(ha[j], ha[j + 8]);
#pragma unroll
        for (int j = 0; j < 4; ++j) ha[j] = tr16(ha[j], ha[j + 4]);
        { const int u = fcc * 8 + (frow & 3) * 2;
          tr_h[u * 2 + (lane >> 5)] = tr8(ha[0], ha[2], b3); tr_h[u * 2 + 2 + (lane >> 5)] = tr8(ha[1], ha[3], b3); }
        const f32x2 t = *reinterpret_cast<const f32x2*>(tr_h + 2 * lane);
        STGF(gr24x2(t[0], t[1], t40_lo, t40_hi), WS_HP + (unsigned)(w * 128 + 2 * lane) * 8);
        STAMP(10);
      }
    } else if (wave == 2) {
#pragma unroll
      for (int k = 0; k < 16; ++k) wo_s[(k * 4 + g4) * 17 + n16] = wt[k];
    }
    __builtin_amdgcn_sched_barrier(0);
    {
      const unsigned qoff = WS_QG + (unsigned)(bat * NQ + kvh * 512 + wave * 128 + 2 * lane) * 8;
      u32x4 qg[4];
      unsigned spins = 0;
      __builtin_amdgcn_s_sleep(Q_DELAY);
#pragma unroll
      for (int k = 0; k < 4; ++k) qg[k] = LDCQ(qoff + k * (2 * NQ * 8));
      for (;;) {
        const bool k0 = __all(ok24x2(qg[0], t40_lo, t40_hi)), k1 = __all(ok24x2(qg[1], t40_lo, t40_hi));
        const bool k2 = __all(ok24x2(qg[2], t40_lo, t40_hi)), k3 = __all(ok24x2(qg[3], t40_lo, t40_hi));
        if ((k0 & k1 & k2 & k3) || ++spins >= SPIN_CAP) break;
        __builtin_amdgcn_s_sleep(8);
        asm volatile("" ::: "memory");
        if (!k0) qg[0] = LDG(qoff);
        if (!k1) qg[1] = LDG(qoff + 1 * (2 * NQ * 8));
        if (!k2) qg[2] = LDG(qoff + 2 * (2 * NQ * 8));
        if (!k3) qg[3] = LDG(qoff + 3 * (2 * NQ * 8));
      }
      q_s[wave * 64 + lane] = cvtpk((val24(qg[0][0]) + val24(qg[1][0])) + (val24(qg[2][0]) + val24(qg[3][0])),
                                    (val24(qg[0][2]) + val24(qg[1][2])) + (val24(qg[2][2]) + val24(qg[3][2])));
      SET_FLAG(4 + wave);
      STAMP(11);
    }
    if (wave == 3) {
    if (f2kb < 28) {
      const unsigned hoff = WS_HP + (unsigned)(f2kb * 128 + 2 * lane) * 8;
      u32x4 hg[8];
      unsigned spins = 0;
#pragma unroll
      for (int k = 0; k < 8; ++k) hg[k] = LDCF(hoff + k * (28 * 128 * 8));
      for (;;) {
        bool ok = true;
#pragma unroll
        for (int k = 0; k < 8; ++k) ok &= ok24x2(hg[k], t40_lo, t40_hi);
        if (__all(ok) || ++spins >= SPIN_CAP) break;
        __builtin_amdgcn_s_sleep(8);
        asm volatile("" ::: "memory");
#pragma unroll
        for (int k = 0; k < 8; ++k) hg[k] = LDG(hoff + k * (28 * 128 * 8));
      }
      float h0 = 0.f, h1 = 0.f;
#pragma unroll
      for (int k = 0; k < 8; ++k) { h0 += val24(hg[k][0]); h1 += val24(hg[k][2]); }
      *reinterpret_cast<f32x2*>(h_s + 2 * lane) = f32x2{fmaxf(rbf(h0), 0.f), fmaxf(rbf(h1), 0.f)};
      float ya[16];
#pragma unroll
      for (int j = 0; j < 16; ++j) ya[j] = 0.f;
#pragma unroll
      for (int k = 0; k < 8; ++k) {
        const f32x2 hv = *reinterpret_cast<const f32x2*>(h_s + (frow + 8 * k) * 2);
#pragma unroll
        for (int j = 0; j < 4; ++j) {
          const float l0 = bf_lo(wt[k][j]), hh = bf_hi(wt[k][j]);
          ya[2 * j] = fmaf(hv[0], l0, ya[2 * j]); ya[2 * j + 1] = fmaf(hv[0], hh, ya[2 * j + 1]);
          ya[8 + 2 * j] = fmaf(hv[1], l0, ya[8 + 2 * j]); ya[8 + 2 * j + 1] = fmaf(hv[1], hh, ya[8 + 2 * j + 1]);
        }
      }
#pragma unroll
      for (int j = 0; j < 8; ++j) ya[j] = tr32(ya[j], ya[j + 8]);
#pragma unroll
      for (int j = 0; j < 4; ++j) ya[j] = tr16(ya[j], ya[j + 4]);
      asm volatile("" ::: "memory");
      *reinterpret_cast<f32x2*>(h_s + ((fcc >> 1) * 2 + (lane >> 5)) * 16 + ((frow & 3) * 2 + (fcc & 1)) * 2) = f32x2{tr8(ya[0], ya[2], b3), tr8(ya[1], ya[3], b3)};
      const f32x2 t = *reinterpret_cast<const f32x2*>(h_s + 2 * lane);
      STGF(gr24x2(t[0], t[1], t40_lo, t40_hi), WS_YP + (unsigned)(f2kb * 1024 + f2cb * 128 + 2 * lane) * 8);
    }
      STAMP(8);
    }
    __builtin_amdgcn_sched_barrier(0);
#pragma unroll
    for (int d = 0; d < HT; ++d) ISSUE(d, sbase + d * TSTEP);
  }

  {
    const unsigned msk0 = (n16 & 3) == 0 ? 0x0000ffffu : (n16 & 3) == 1 ? 0xffff0000u : 0u;
    const unsigned msk1 = (n16 & 3) == 2 ? 0x0000ffffu : (n16 & 3) == 3 ? 0xffff0000u : 0u;
    float m_run = -INFINITY, l_run = 0.f;
    f32x4 olo = {0.f, 0.f, 0.f, 0.f}, ohi = {0.f, 0.f, 0.f, 0.f};
    while (!__all(GET_FLAG(4 + (lane & 3)))) __builtin_amdgcn_s_sleep(1);
    bf16x8 qf[4];
#pragma unroll
    for (int s = 0; s < 4; ++s) qf[s] = *reinterpret_cast<const bf16x8*>(q_s + ((n16 >> 2) * DH + s * 32 + g4 * 8) / 2);
    STAMP(2);
#define TILE(d) do { \
      f32x4 sc = {0.f, 0.f, 0.f, 0.f}; \
      _Pragma("unroll") for (int s = 0; s < 4; ++s) sc = __builtin_amdgcn_mfma_f32_16x16x32_bf16(__builtin_bit_cast(bf16x8, kb[d][s]), qf[s], sc, 0, 0, 0); \
      float tmax = fmaxf(fmaxf(sc[0], sc[1]), fmaxf(sc[2], sc[3])); \
      tmax = xrow_max(tmax); \
      const float mnew = fmaxf(m_run, tmax); \
      const float alpha = __builtin_amdgcn_exp2f((m_run - mnew) * SOFT_C); \
      const float mc = -mnew * SOFT_C; \
      float pr[4]; \
      _Pragma("unroll") for (int r = 0; r < 4; ++r) pr[r] = __builtin_amdgcn_exp2f(fmaf(sc[r], SOFT_C, mc)); \
      l_run = fmaf(l_run, alpha, (pr[0] + pr[1]) + (pr[2] + pr[3])); \
      m_run = mnew; \
      olo *= alpha; ohi *= alpha; \
      _Pragma("unroll") for (int r = 0; r < 4; ++r) { \
        const unsigned pk = cvtpk(pr[r], pr[r]); \
        const u32x2 bp = {pk & msk0, pk & msk1}; \
        const u32x2 vlo = {vb[d][r][0], vb[d][r][1]}, vhi = {vb[d][r][2], vb[d][r][3]}; \
        olo = __builtin_amdgcn_mfma_f32_16x16x16bf16_1k(__builtin_bit_cast(s16x4, vlo), __builtin_bit_cast(s16x4, bp), olo, 0, 0, 0); \
        ohi = __builtin_amdgcn_mfma_f32_16x16x16bf16_1k(__builtin_bit_cast(s16x4, vhi), __builtin_bit_cast(s16x4, bp), ohi, 0, 0, 0); \
      } \
      __builtin_amdgcn_sched_barrier(0); } while (0)
    if (streamer) {
#pragma unroll
      for (int d = 0; d < REM; ++d) { TILE(d); ISSUE(d, sbase + (unsigned)(DEPTH + d) * TSTEP); }
#pragma unroll 1
      for (int it = 0; it < NFULL - 1; ++it) {
        const unsigned snext = sbase + (unsigned)(REM + (it + 1) * DEPTH) * TSTEP;
#pragma unroll
        for (int d = 0; d < DEPTH; ++d) {
          TILE((d + REM) % DEPTH);
          ISSUE((d + REM) % DEPTH, snext + d * TSTEP);
        }
      }
#pragma unroll
      for (int d = 0; d < DEPTH; ++d) TILE((d + REM) % DEPTH);
    } else {
#pragma unroll
      for (int d = 0; d < HT; ++d) TILE(d);
      if (HT == 0) m_run = -3.0e38f;
    }
#undef TILE
#undef ISSUE
    STAMP(3);
    l_run = xrow_sum(l_run);
    float* mo = mrg_o + wave * 512 + (n16 >> 2) * DH + g4 * 32 + (n16 & 3);
#pragma unroll
    for (int r = 0; r < 4; ++r) { mo[r * 8] = olo[r]; mo[r * 8 + 4] = ohi[r]; }
    if (g4 == 0 && (n16 & 3) == 0) { mrg_m[wave * 4 + (n16 >> 2)] = m_run; mrg_l[wave * 4 + (n16 >> 2)] = l_run; }
  }
  const unsigned t40_lo = (unsigned)(tag << 24), t40_hi = (unsigned)(tag >> 8);
  asm volatile("" :: "v"(t40_lo), "v"(t40_hi));
  __syncthreads();
  {
    const int h = tid >> 7;
    float mw[8], M = -INFINITY;
#pragma unroll
    for (int v = 0; v < 8; ++v) { mw[v] = mrg_m[v * 4 + h]; M = fmaxf(M, mw[v]); }
    float o = 0.f, L = 0.f;
#pragma unroll
    for (int v = 0; v < 8; ++v) {
      const float f = __builtin_amdgcn_exp2f((mw[v] - M) * SOFT_C);
      o = fmaf(f, mrg_o[v * 512 + tid], o); L = fmaf(f, mrg_l[v * 4 + h], L);
    }
    __builtin_amdgcn_raw_buffer_store_b64(gr24(f24(o), t40_lo, t40_hi), wsr, WS_AP + (unsigned)(w * 512 + tid) * 8, 0, AUX_SC1);
    if ((tid & 127) < 2) {
      const unsigned mb = __float_as_uint(M);
      const unsigned p0 = (tid & 1) ? f24(L) : (mb >> 8), p1 = (tid & 1) ? 0u : (mb & 0xffu);
      STG((u32x4{p0 | t40_lo, t40_hi, p1 | t40_lo, t40_hi}), WS_ST + (unsigned)(((kvh * 4 + h) * 32 + bat * 16 + split) * 4 + 2 * (tid & 1)) * 8);
    }
  }
  STAMP(4);
  if (w < 64 && wave == 1) {
      const unsigned yoff = WS_YP + (unsigned)(frow * 1024 + w * 16 + fcc * 2) * 8;
      u32x4 yg[4];
      unsigned spins = 0;
      for (;;) {
        bool ok = true;
#pragma unroll
        for (int k = 0; k < 4; ++k) yg[k] = (k < 3 || lane < 32) ? LDGF(yoff + k * 8 * 8192) : u32x4{t40_lo, t40_hi, t40_lo, t40_hi};
#pragma unroll
        for (int k = 0; k < 4; ++k) ok &= ok24x2(yg[k], t40_lo, t40_hi);
        if (__all(ok) || ++spins >= SPIN_CAP) break;
        asm volatile("" ::: "memory");
      }
      float y0 = (val24(yg[0][0]) + val24(yg[1][0])) + (val24(yg[2][0]) + val24(yg[3][0]));
      float y1 = (val24(yg[0][2]) + val24(yg[1][2])) + (val24(yg[2][2]) + val24(yg[3][2]));
      y0 += dpp<ROR8>(y0); y0 = rbf(xrow_sum(y0));
      y1 += dpp<ROR8>(y1); y1 = rbf(xrow_sum(y1));
      if (lane < 8) *reinterpret_cast<f32x2*>(g_s + lane * 2) = f32x2{rbf(y0 / (1.f + __expf(-y0))), rbf(y1 / (1.f + __expf(-y1)))};
    }
  float wof[16];
  {
    const u32x4 woa = wo_s[(lane >> 1) * 17 + wave * 2 + (lane & 1)], wob = wo_s[(32 + (lane >> 1)) * 17 + wave * 2 + (lane & 1)];
#pragma unroll
    for (int j = 0; j < 4; ++j) { wof[2 * j] = bf_lo(woa[j]); wof[2 * j + 1] = bf_hi(woa[j]); wof[8 + 2 * j] = bf_lo(wob[j]); wof[8 + 2 * j + 1] = bf_hi(wob[j]); }
  }
  {
    if (wave == 0) {
      const unsigned o2 = WS_ST + (unsigned)((kvh * 4 + (idx >> 3)) * 128 + lane * 2) * 8;
      u32x4 gs;
      unsigned spins = 0;
      for (;;) {
        gs = LDG(o2);
        const int missing = __popcll(__ballot(!ok24x2(gs, t40_lo, t40_hi)));
        if (missing <= 2 * EARLY_GO) SET_FLAG(8);
        if (missing == 0 || ++spins >= SPIN_CAP) break;
        asm volatile("" ::: "memory");
      }
      SET_FLAG(8);
      const float mine = __uint_as_float((gs[0] << 8) | ((lane & 1) ? 0u : (gs[2] & 0xffu)));
      const float other = dpp<XOR1>(mine);
      const float m = (lane & 1) ? other : mine, l = (lane & 1) ? mine : other;
      float M = m; M = fmaxf(M, dpp<XOR2>(M)); M = fmaxf(M, dpp<XOR7>(M)); M = fmaxf(M, dpp<ROR8>(M));
      { auto t = __builtin_amdgcn_permlane16_swap(__float_as_uint(M), __float_as_uint(M), false, false); M = fmaxf(__uint_as_float(t[0]), __uint_as_float(t[1])); }
      const float f = __builtin_amdgcn_exp2f((m - M) * SOFT_C);
      float L = f * l; L += dpp<XOR2>(L); L += dpp<XOR7>(L); L += dpp<ROR8>(L); L = tr16(L, L);
      if ((lane & 1) == 0) c_scl[lane >> 1] = f / L;
    } else {
      while (!GET_FLAG(8)) __builtin_amdgcn_s_sleep(0);
    }
    const int pp0 = wave * 4 + (lane >> 5), pp1 = pp0 + 2;
    const unsigned o1 = WS_AP + (unsigned)((kvh + 8 * (pp0 >> 4) + 16 * (pp0 & 15)) * 512 + (idx >> 2) * 64 + (lane & 31) * 2) * 8;
    u32x4 ga, gb;
    unsigned spins = 0;
    for (;;) {
      ga = LDG(o1); gb = LDG(o1 + 2 * 16 * 512 * 8);
      if (__all(ok24x2(ga, t40_lo, t40_hi) & ok24x2(gb, t40_lo, t40_hi)) || ++spins >= SPIN_CAP) break;
      asm volatile("" ::: "memory");
    }
    *reinterpret_cast<f32x2*>(c_acc + pp0 * 64 + (lane & 31) * 2) = f32x2{val24(ga[0]), val24(ga[2])};
    *reinterpret_cast<f32x2*>(c_acc + pp1 * 64 + (lane & 31) * 2) = f32x2{val24(gb[0]), val24(gb[2])};
  }
  __syncthreads();
  STAMP(5);
  {
    float o0 = 0.f, o1 = 0.f;
#pragma unroll
    for (int s4 = 0; s4 < 4; ++s4) {
      const f32x4 sa = *reinterpret_cast<const f32x4*>(c_scl + s4 * 4), sb = *reinterpret_cast<const f32x4*>(c_scl + 16 + s4 * 4);
#pragma unroll
      for (int j = 0; j < 4; ++j) { o0 = fmaf(sa[j], c_acc[(s4 * 4 + j) * 64 + lane], o0); o1 = fmaf(sb[j], c_acc[(16 + s4 * 4 + j) * 64 + lane], o1); }
    }
    o0 = rbf(o0); o1 = rbf(o1);
    const float o00 = __shfl(o0, lane >> 1), o01 = __shfl(o0, 32 + (lane >> 1)), o10 = __shfl(o1, lane >> 1), o11 = __shfl(o1, 32 + (lane >> 1));
    float sa[8];
#pragma unroll
    for (int j = 0; j < 8; ++j) sa[j] = tr32(fmaf(o01, wof[8 + j], o00 * wof[j]), fmaf(o11, wof[8 + j], o10 * wof[j]));
#pragma unroll
    for (int j = 0; j < 4; ++j) sa[j] = tr16(sa[j], sa[j + 4]);
    const bool b1 = (lane >> 1) & 1;
    float r0 = (b1 ? sa[2] : sa[0]) + dpp<XOR2>(b1 ? sa[0] : sa[2]), r1 = (b1 ? sa[3] : sa[1]) + dpp<XOR2>(b1 ? sa[1] : sa[3]);
    r0 += dpp<ROR4>(r0); r0 += dpp<ROR8>(r0);
    r1 += dpp<ROR4>(r1); r1 += dpp<ROR8>(r1);
    if ((lane & 12) == 0)
      STG(gr24x2(r0, r1, t40_lo, t40_hi),
          WS_SP + (unsigned)((kvh * 8 + (idx >> 2)) * 1024 + (((idx & 3) * 8 + wave) * 2 + (lane >> 5)) * 16 + (((lane >> 4) & 1) * 4 + (lane & 3)) * 2) * 8);
  }
  STAMP(6);
  if (w < 64) {
    const unsigned soff = WS_SP + (unsigned)((wave * 8 + frow) * 1024 + w * 16 + fcc * 2) * 8;
    const int oidx = (w & 1) * DM + (w >> 1) * 16 + (fcc & 1) * 8 + (fcc >> 2) * 4 + ((fcc >> 1) & 1) * 2;
    const unsigned xr = *reinterpret_cast<const unsigned*>(p.x + oidx);
    u32x4 sg;
    unsigned spins = 0;
    for (;;) {
      sg = LDG(soff);
      if (__all(ok24x2(sg, t40_lo, t40_hi)) || ++spins >= SPIN_CAP) break;
      asm volatile("" ::: "memory");
    }
    float s0 = val24(sg[0]), s1 = val24(sg[2]);
    s0 += dpp<ROR8>(s0); s0 = xrow_sum(s0);
    s1 += dpp<ROR8>(s1); s1 = xrow_sum(s1);
    if (lane < 8) *reinterpret_cast<f32x2*>(fin_s + wave * 16 + lane * 2) = f32x2{s0, s1};
    __syncthreads();
    if (wave == 0 && lane < 8) {
      float sa = 0.f, sb = 0.f;
#pragma unroll
      for (int v = 0; v < 8; ++v) { const f32x2 t = *reinterpret_cast<const f32x2*>(fin_s + v * 16 + lane * 2); sa += t[0]; sb += t[1]; }
      const f32x2 g = *reinterpret_cast<const f32x2*>(g_s + lane * 2);
      const float r0 = bf_lo(xr) + rbf(rbf(sa) * g[0]), r1 = bf_hi(xr) + rbf(rbf(sb) * g[1]);
      *reinterpret_cast<unsigned*>(p.out + oidx) = cvtpk(r0, r1);
      if (w == 0 && lane == 0) __hip_atomic_store(reinterpret_cast<u64*>(p.ws + WS_EPOCH), tag, __ATOMIC_RELAXED, __HIP_MEMORY_SCOPE_AGENT);
    }
  }
  STAMP(7);
}

}

extern "C" void kernel_launch(void* const* d_in, const int* in_sizes, int n_in, void* d_out, int out_size, void* d_ws, size_t ws_size,
                              hipStream_t stream) {
  Params p;
  p.x = (const unsigned short*)d_in[0];
  p.k = (const unsigned short*)d_in[1];
  p.v = (const unsigned short*)d_in[2];
  p.wq = (const unsigned short*)d_in[3];
  p.wo = (const unsigned short*)d_in[4];
  p.w1 = (const unsigned short*)d_in[6];
  p.w2 = (const unsigned short*)d_in[7];
  p.out = (unsigned short*)d_out;
  p.ws = (unsigned char*)d_ws;
  p.dbg = (unsigned char*)d_ws;
  hipLaunchKernelGGL(fused_decode_kernel, dim3(NWG), dim3(NTHR), 0, stream, p);
}
```

```cpp
#include <hip/hip_runtime.h>
#include <hip/hip_bf16.h>
#include <stdint.h>

typedef unsigned u32x4 __attribute__((ext_vector_type(4)));
typedef unsigned u32x2 __attribute__((ext_vector_type(2)));
typedef float f32x4 __attribute__((ext_vector_type(4)));
typedef float f32x2 __attribute__((ext_vector_type(2)));
typedef __bf16 bf16x2 __attribute__((ext_vector_type(2)));
typedef __bf16 bf16x8 __attribute__((ext_vector_type(8)));
typedef short s16x4 __attribute__((ext_vector_type(4)));
typedef unsigned long long u64;

namespace {

constexpr int NWG = 256, NTHR = 512, NWAVE = 8;
constexpr int SK = 32768, DH = 128, DM = 512, DFF = 1792, NQ = 4096;
#define DEPTH_ 6
constexpr int DEPTH = DEPTH_;
#define HT DEPTH
constexpr int NFULL = (32 - HT) / DEPTH, REM = (32 - HT) % DEPTH;
static_assert(4 * (NFULL * DEPTH + REM) + 4 * HT == 128, "128 tiles of 16 keys per workgroup");
constexpr int NFF = 224;
constexpr unsigned WS_EPOCH = 0;
constexpr unsigned WS_QG = 4096;
constexpr unsigned WS_HP = WS_QG + 4 * 2 * NQ * 8;
constexpr unsigned WS_AP = WS_HP + 8 * 28 * 128 * 8;
constexpr unsigned WS_ST = WS_AP + NWG * 4 * 128 * 8;
constexpr unsigned WS_YP = WS_ST + 8 * 4 * 32 * 4 * 8;
constexpr unsigned WS_SP = WS_YP + 28 * 1024 * 8;
constexpr unsigned WS_END = WS_SP + 64 * 1024 * 8;
constexpr unsigned WS_DBG = 8u << 20;
constexpr unsigned SPIN_CAP = 1u << 16;
constexpr int AUX_NT = 2, AUX_SC1 = 16;
constexpr float SOFT_C = 0.08838834764831845f * 1.4426950408889634f;

__device__ __forceinline__ float bf_lo(unsigned u) { return __uint_as_float(u << 16); }
__device__ __forceinline__ float bf_hi(unsigned u) { return __uint_as_float(u & 0xffff0000u); }
__device__ __forceinline__ unsigned cvtpk(float lo, float hi) {
  return __builtin_bit_cast(unsigned, __builtin_convertvector(f32x2{lo, hi}, bf16x2));
}
__device__ __forceinline__ float rbf(float x) { return bf_lo(cvtpk(x, x)); }
template <int CTRL> __device__ __forceinline__ float dpp(float x) {
  return __builtin_bit_cast(float, __builtin_amdgcn_mov_dpp(__builtin_bit_cast(int, x), CTRL, 0xf, 0xf, true));
}
constexpr int ROR4 = 0x124, ROR8 = 0x128, ROR12 = 0x12C, XOR1 = 0xB1, XOR2 = 0x4E, XOR7 = 0x141;
__device__ __forceinline__ float xrow_max(float x) {
  auto s = __builtin_amdgcn_permlane16_swap(__float_as_uint(x), __float_as_uint(x), false, false);
  x = fmaxf(__uint_as_float(s[0]), __uint_as_float(s[1]));
  auto t = __builtin_amdgcn_permlane32_swap(__float_as_uint(x), __float_as_uint(x), false, false);
  return fmaxf(__uint_as_float(t[0]), __uint_as_float(t[1]));
}
__device__ __forceinline__ float xrow_sum(float x) {
  auto s = __builtin_amdgcn_permlane16_swap(__float_as_uint(x), __float_as_uint(x), false, false);
  x = __uint_as_float(s[0]) + __uint_as_float(s[1]);
  auto t = __builtin_amdgcn_permlane32_swap(__float_as_uint(x), __float_as_uint(x), false, false);
  return __uint_as_float(t[0]) + __uint_as_float(t[1]);
}
__device__ __forceinline__ float wave_sum(float x) {
  x += dpp<XOR1>(x); x += dpp<XOR2>(x); x += dpp<XOR7>(x); x += dpp<ROR8>(x);
  return xrow_sum(x);
}
__device__ __forceinline__ float tr32(float a, float b) {
  auto t = __builtin_amdgcn_permlane32_swap(__float_as_uint(a), __float_as_uint(b), false, false);
  return __uint_as_float(t[0]) + __uint_as_float(t[1]);
}
__device__ __forceinline__ float tr16(float a, float b) {
  auto t = __builtin_amdgcn_permlane16_swap(__float_as_uint(a), __float_as_uint(b), false, false);
  return __uint_as_float(t[0]) + __uint_as_float(t[1]);
}
__device__ __forceinline__ float tr8(float a, float b, bool bit3) {
  const float keep = bit3 ? b : a, send = bit3 ? a : b;
  return keep + dpp<ROR8>(send);
}
__device__ __forceinline__ unsigned f24(float x) { return (__float_as_uint(x) + 0x80u) >> 8; }
__device__ __forceinline__ u32x2 gr24(unsigned payload24, unsigned tlo8, unsigned thi) { return u32x2{payload24 | tlo8, thi}; }
__device__ __forceinline__ bool ok24(unsigned lo, unsigned hi, unsigned tlo8, unsigned thi) { return ((lo & 0xff000000u) == tlo8) & (hi == thi); }
__device__ __forceinline__ float val24(unsigned lo) { return __uint_as_float(lo << 8); }

struct Params {
  const unsigned short* x; const unsigned short* k; const unsigned short* v; const unsigned short* wq; const unsigned short* wo;
  const unsigned short* w1; const unsigned short* w2; unsigned short* out; unsigned char* ws;
};

#define STAMP(i) do {} while (0)
#define Q_DELAY 32
#define POLL_SLEEP 16
#define EARLY_GO 3
#define PRE_BAR 2
#define LDNT(p_) __builtin_nontemporal_load(reinterpret_cast<const u32x4*>(p_))

__device__ __forceinline__ void layernorm_to_lds(const u32x4 xa, const u32x4 xb, float* dst, int lane) {
  float e0[8], e1[8];
#pragma unroll
  for (int j = 0; j < 4; ++j) { e0[2 * j] = bf_lo(xa[j]); e0[2 * j + 1] = bf_hi(xa[j]); e1[2 * j] = bf_lo(xb[j]); e1[2 * j + 1] = bf_hi(xb[j]); }
  float s0 = 0.f, s1 = 0.f;
#pragma unroll
  for (int j = 0; j < 8; ++j) { s0 += e0[j]; s1 += e1[j]; }
  s0 = wave_sum(s0) * (1.f / DM); s1 = wave_sum(s1) * (1.f / DM);
  float v0 = 0.f, v1 = 0.f;
#pragma unroll
  for (int j = 0; j < 8; ++j) { const float a = e0[j] - s0, b = e1[j] - s1; v0 = fmaf(a, a, v0); v1 = fmaf(b, b, v1); }
  v0 = wave_sum(v0) * (1.f / DM); v1 = wave_sum(v1) * (1.f / DM);
  const float m0 = rbf(s0), m1 = rbf(s1);
  const float d0 = 1.f / rbf(sqrtf(rbf(rbf(v0) + 1e-5f))), d1 = 1.f / rbf(sqrtf(rbf(rbf(v1) + 1e-5f)));
#pragma unroll
  for (int j = 0; j < 8; j += 2)
    *reinterpret_cast<f32x4*>(dst + (8 * lane + j) * 2) =
        f32x4{rbf(rbf(e0[j] - m0) * d0), rbf(rbf(e1[j] - m1) * d1), rbf(rbf(e0[j + 1] - m0) * d0), rbf(rbf(e1[j + 1] - m1) * d1)};
}

__device__ __forceinline__ bool ok24x2(const u32x4 g, unsigned tlo8, unsigned thi) { return ok24(g[0], g[1], tlo8, thi) & ok24(g[2], g[3], tlo8, thi); }
__device__ __forceinline__ u32x4 gr24x2(float a, float b, unsigned tlo8, unsigned thi) { return u32x4{f24(a) | tlo8, thi, f24(b) | tlo8, thi}; }

__global__ void __launch_bounds__(NTHR, 2) fused_decode_kernel(Params p) {
  __shared__ __attribute__((aligned(16))) float lds[13312];
  float* xn_s = lds;
  float* red_q = lds + 1024;
  float* mrg_m = lds + 1536;
  float* mrg_l = lds + 1568;
  unsigned short* o_s = reinterpret_cast<unsigned short*>(lds + 2560);
  float* fin_s = lds + 1632;
  float* tr_h = lds + 1760;
  float* h_s = lds + 1888;
  float* g_s = lds + 2016;
  unsigned* q_s = reinterpret_cast<unsigned*>(lds + 2048);
  unsigned* flag_w = reinterpret_cast<unsigned*>(lds + 2304);
  float* mrg_o = lds + 4608;
  u32x4* wo_s = reinterpret_cast<u32x4*>(lds + 8704);

  const int w = blockIdx.x, tid = threadIdx.x, lane = tid & 63;
  const int wave = __builtin_amdgcn_readfirstlane(tid >> 6);
  const int kvh = w & 7, bat = (w >> 3) & 1, split = w >> 4, idx = w >> 3;
  const int n16 = lane & 15, g4 = lane >> 4, frow = lane >> 3, fcc = lane & 7;
  STAMP(0);

  const __amdgpu_buffer_rsrc_t wsr = __builtin_amdgcn_make_buffer_rsrc((void*)p.ws, 0, WS_END, 0x00020000);
#define LOAD_EPOCH() __hip_atomic_load(reinterpret_cast<const u64*>(p.ws + WS_EPOCH), __ATOMIC_RELAXED, __HIP_MEMORY_SCOPE_AGENT)
#define WG_BARRIER() do { __builtin_amdgcn_sched_barrier(0); asm volatile("s_waitcnt lgkmcnt(0)\n\ts_barrier" ::: "memory"); __builtin_amdgcn_sched_barrier(0); } while (0)
#define LDG(off_) __builtin_amdgcn_raw_buffer_load_b128(wsr, (off_), 0, AUX_SC1)
#define STG(v_, off_) __builtin_amdgcn_raw_buffer_store_b128((v_), wsr, (off_), 0, AUX_SC1)
  const bool streamer = wave >= 4;
  const int f2kb = (w & 7) + 8 * (w >> 6), f2cb = (w >> 3) & 7;

#define SET_FLAG(i_) __hip_atomic_store(&flag_w[i_], 1u, __ATOMIC_RELEASE, __HIP_MEMORY_SCOPE_WORKGROUP)
#define GET_FLAG(i_) (__hip_atomic_load(&flag_w[i_], __ATOMIC_ACQUIRE, __HIP_MEMORY_SCOPE_WORKGROUP) != 0u)
  const __amdgpu_buffer_rsrc_t kr = __builtin_amdgcn_make_buffer_rsrc((void*)p.k, 0, 2u * 8 * SK * DH * 2, 0x00020000);
  const __amdgpu_buffer_rsrc_t vr = __builtin_amdgcn_make_buffer_rsrc((void*)p.v, 0, 2u * 8 * SK * DH * 2, 0x00020000);
  const unsigned kvoff = (unsigned)(((n16 & 3) * 4 + (n16 >> 2)) * 256 + g4 * 16);
  const unsigned vvoff = (unsigned)lane * 16;
  const unsigned sbase = (unsigned)((bat * 8 + kvh) * SK + split * 2048 + ((streamer ? 4 * HT : 0) + (wave & 3)) * 16) * (DH * 2);
  constexpr unsigned TSTEP = 4 * 16 * DH * 2;
  u32x4 kb[DEPTH][4], vb[DEPTH][4];
#define ISSUE(d_, so_) do { \
    _Pragma("unroll") for (int s_ = 0; s_ < 4; ++s_) kb[d_][s_] = __builtin_amdgcn_raw_buffer_load_b128(kr, kvoff + s_ * 64, (so_), AUX_NT); \
    _Pragma("unroll") for (int s_ = 0; s_ < 4; ++s_) vb[d_][s_] = __builtin_amdgcn_raw_buffer_load_b128(vr, vvoff + s_ * 1024, (so_), AUX_NT); \
    __builtin_amdgcn_sched_barrier(0); } while (0)
  u64 tag;
  if (__builtin_expect(streamer, 1)) {
    STAMP(9);
#pragma unroll
    for (int d = 0; d < PRE_BAR; ++d) ISSUE(d, sbase + d * TSTEP);
    WG_BARRIER();
#pragma unroll
    for (int d = PRE_BAR; d < DEPTH; ++d) ISSUE(d, sbase + d * TSTEP);
    tag = LOAD_EPOCH() + 1;
  } else {
    const u32x4 xa = *reinterpret_cast<const u32x4*>(p.x + 8 * lane);
    const u32x4 xb = *reinterpret_cast<const u32x4*>(p.x + DM + 8 * lane);
    tag = LOAD_EPOCH() + 1;
    __builtin_amdgcn_sched_barrier(0);
    const int qrow = (w >> 6) * 128 + wave * 32 + frow;
    const unsigned short* wqp = p.wq + (size_t)qrow * NQ + (w & 63) * 64 + fcc * 8;
    u32x4 wv[4];
#pragma unroll
    for (int k = 0; k < 4; ++k) wv[k] = LDNT(wqp + (size_t)k * 8 * NQ);
    __builtin_amdgcn_sched_barrier(0);
    const bool has_ff = w < NFF;
    const int f1row = (w / 28) * 64 + frow;
    u32x4 wt[16];
    if (wave == 1) {
      if (has_ff) {
#pragma unroll
        for (int k = 0; k < 8; ++k) wt[k] = LDNT(p.w1 + (size_t)(f1row + 8 * k) * DFF + (w % 28) * 64 + fcc * 8);
      }
    } else if (wave == 2) {
#pragma unroll
      for (int k = 0; k < 16; ++k) wt[k] = LDNT(p.wo + (size_t)(kvh * 512 + (idx >> 2) * 64 + k * 4 + g4) * DM + (idx & 3) * 128 + n16 * 8);
    } else if (wave == 3) {
      if (f2kb < 28) {
#pragma unroll
        for (int k = 0; k < 8; ++k) wt[k] = LDNT(p.w2 + (size_t)(f2kb * 64 + frow + 8 * k) * DM + f2cb * 64 + fcc * 8);
      }
    }
    if (tid < 16) flag_w[tid] = 0u;
    WG_BARRIER();
    layernorm_to_lds(xa, xb, xn_s, lane);
    const unsigned t40_lo = (unsigned)(tag << 24), t40_hi = (unsigned)(tag >> 8);
    const bool b3 = (lane >> 3) & 1;
    {
      float qa[16];
#pragma unroll
      for (int j = 0; j < 16; ++j) qa[j] = 0.f;
#pragma unroll
      for (int k = 0; k < 4; ++k) {
        const f32x2 xv = *reinterpret_cast<const f32x2*>(xn_s + (qrow + 8 * k) * 2);
#pragma unroll
        for (int j = 0; j < 4; ++j) {
          const float l0 = bf_lo(wv[k][j]), h0 = bf_hi(wv[k][j]);
          qa[2 * j] = fmaf(xv[0], l0, qa[2 * j]); qa[2 * j + 1] = fmaf(xv[0], h0, qa[2 * j + 1]);
          qa[8 + 2 * j] = fmaf(xv[1], l0, qa[8 + 2 * j]); qa[8 + 2 * j + 1] = fmaf(xv[1], h0, qa[8 + 2 * j + 1]);
        }
      }
#pragma unroll
      for (int j = 0; j < 8; ++j) qa[j] = tr32(qa[j], qa[j + 8]);
#pragma unroll
      for (int j = 0; j < 4; ++j) qa[j] = tr16(qa[j], qa[j + 4]);
      *reinterpret_cast<f32x2*>(red_q + wave * 128 + (lane >> 5) * 64 + fcc * 8 + ((lane >> 3) & 3) * 2) = f32x2{tr8(qa[0], qa[2], b3), tr8(qa[1], qa[3], b3)};
      SET_FLAG(wave);
    }
    if (wave == 0) {
      while (!__all(GET_FLAG(lane & 3))) __builtin_amdgcn_s_sleep(0);
      f32x2 t = *reinterpret_cast<const f32x2*>(red_q + 2 * lane);
#pragma unroll
      for (int v = 1; v < 4; ++v) t += *reinterpret_cast<const f32x2*>(red_q + v * 128 + 2 * lane);
      STG(gr24x2(t[0], t[1], t40_lo, t40_hi), WS_QG + (unsigned)(((w >> 6) * 2 + (lane >> 5)) * NQ + (w & 63) * 64 + (lane & 31) * 2) * 8);
      STAMP(1);
    } else if (wave == 1) {
      if (has_ff) {
        float ha[16];
#pragma unroll
        for (int j = 0; j < 16; ++j) ha[j] = 0.f;
#pragma unroll
        for (int k = 0; k < 8; ++k) {
          const f32x2 xv = *reinterpret_cast<const f32x2*>(xn_s + (f1row + 8 * k) * 2);
#pragma unroll
          for (int j = 0; j < 4; ++j) {
            const float l0 = bf_lo(wt[k][j]), h0 = bf_hi(wt[k][j]);
            ha[2 * j] = fmaf(xv[0], l0, ha[2 * j]); ha[2 * j + 1] = fmaf(xv[0], h0, ha[2 * j + 1]);
            ha[8 + 2 * j] = fmaf(xv[1], l0, ha[8 + 2 * j]); ha[8 + 2 * j + 1] = fmaf(xv[1], h0, ha[8 + 2 * j + 1]);
          }
        }
#pragma unroll
        for (int j = 0; j < 8; ++j) ha[j] = tr32(ha[j], ha[j + 8]);
#pragma unroll
        for (int j = 0; j < 4; ++j) ha[j] = tr16(ha[j], ha[j + 4]);
        { const int u = fcc * 8 + (frow & 3) * 2;
          tr_h[u * 2 + (lane >> 5)] = tr8(ha[0], ha[2], b3); tr_h[u * 2 + 2 + (lane >> 5)] = tr8(ha[1], ha[3], b3); }
        const f32x2 t = *reinterpret_cast<const f32x2*>(tr_h + 2 * lane);
        STG(gr24x2(t[0], t[1], t40_lo, t40_hi), WS_HP + (unsigned)(w * 128 + 2 * lane) * 8);
        STAMP(10);
      }
    } else if (wave == 2) {
#pragma unroll
      for (int k = 0; k < 16; ++k) wo_s[(k * 4 + g4) * 17 + n16] = wt[k];
    }
    __builtin_amdgcn_sched_barrier(0);
    {
      const unsigned qoff = WS_QG + (unsigned)(bat * NQ + kvh * 512 + wave * 128 + 2 * lane) * 8;
      u32x4 qg[4];
      unsigned spins = 0;
      __builtin_amdgcn_s_sleep(Q_DELAY);
#pragma unroll
      for (int k = 0; k < 4; ++k) qg[k] = LDG(qoff + k * (2 * NQ * 8));
      for (;;) {
        const bool k0 = __all(ok24x2(qg[0], t40_lo, t40_hi)), k1 = __all(ok24x2(qg[1], t40_lo, t40_hi));
        const bool k2 = __all(ok24x2(qg[2], t40_lo, t40_hi)), k3 = __all(ok24x2(qg[3], t40_lo, t40_hi));
        if ((k0 & k1 & k2 & k3) || ++spins >= SPIN_CAP) break;
        __builtin_amdgcn_s_sleep(8);
        asm volatile("" ::: "memory");
        if (!k0) qg[0] = LDG(qoff);
        if (!k1) qg[1] = LDG(qoff + 1 * (2 * NQ * 8));
        if (!k2) qg[2] = LDG(qoff + 2 * (2 * NQ * 8));
        if (!k3) qg[3] = LDG(qoff + 3 * (2 * NQ * 8));
      }
      q_s[wave * 64 + lane] = cvtpk((val24(qg[0][0]) + val24(qg[1][0])) + (val24(qg[2][0]) + val24(qg[3][0])),
                                    (val24(qg[0][2]) + val24(qg[1][2])) + (val24(qg[2][2]) + val24(qg[3][2])));
      SET_FLAG(4 + wave);
      STAMP(11);
    }
    if (wave == 3) {
    if (f2kb < 28) {
      const unsigned hoff = WS_HP + (unsigned)(f2kb * 128 + 2 * lane) * 8;
      u32x4 hg[8];
      unsigned spins = 0;
#pragma unroll
      for (int k = 0; k < 8; ++k) hg[k] = LDG(hoff + k * (28 * 128 * 8));
      for (;;) {
        bool ok = true;
#pragma unroll
        for (int k = 0; k < 8; ++k) ok &= ok24x2(hg[k], t40_lo, t40_hi);
        if (__all(ok) || ++spins >= SPIN_CAP) break;
        __builtin_amdgcn_s_sleep(8);
        asm volatile("" ::: "memory");
#pragma unroll
        for (int k = 0; k < 8; ++k) hg[k] = LDG(hoff + k * (28 * 128 * 8));
      }
      float h0 = 0.f, h1 = 0.f;
#pragma unroll
      for (int k = 0; k < 8; ++k) { h0 += val24(hg[k][0]); h1 += val24(hg[k][2]); }
      *reinterpret_cast<f32x2*>(h_s + 2 * lane) = f32x2{fmaxf(rbf(h0), 0.f), fmaxf(rbf(h1), 0.f)};
      float ya[16];
#pragma unroll
      for (int j = 0; j < 16; ++j) ya[j] = 0.f;
#pragma unroll
      for (int k = 0; k < 8; ++k) {
        const f32x2 hv = *reinterpret_cast<const f32x2*>(h_s + (frow + 8 * k) * 2);
#pragma unroll
        for (int j = 0; j < 4; ++j) {
          const float l0 = bf_lo(wt[k][j]), hh = bf_hi(wt[k][j]);
          ya[2 * j] = fmaf(hv[0], l0, ya[2 * j]); ya[2 * j + 1] = fmaf(hv[0], hh, ya[2 * j + 1]);
          ya[8 + 2 * j] = fmaf(hv[1], l0, ya[8 + 2 * j]); ya[8 + 2 * j + 1] = fmaf(hv[1], hh, ya[8 + 2 * j + 1]);
        }
      }
#pragma unroll
      for (int j = 0; j < 8; ++j) ya[j] = tr32(ya[j], ya[j + 8]);
#pragma unroll
      for (int j = 0; j < 4; ++j) ya[j] = tr16(ya[j], ya[j + 4]);
      asm volatile("" ::: "memory");
      *reinterpret_cast<f32x2*>(h_s + ((fcc >> 1) * 2 + (lane >> 5)) * 16 + (fcc & 1) * 8 + (frow & 3) * 2) = f32x2{tr8(ya[0], ya[2], b3), tr8(ya[1], ya[3], b3)};
      const f32x2 t = *reinterpret_cast<const f32x2*>(h_s + 2 * lane);
      STG(gr24x2(t[0], t[1], t40_lo, t40_hi), WS_YP + (unsigned)(f2kb * 1024 + f2cb * 128 + 2 * lane) * 8);
    }
      STAMP(8);
    }
    __builtin_amdgcn_sched_barrier(0);
#pragma unroll
    for (int d = 0; d < HT; ++d) ISSUE(d, sbase + d * TSTEP);
  }

  {
    const unsigned msk0 = (n16 & 3) == 0 ? 0x0000ffffu : (n16 & 3) == 1 ? 0xffff0000u : 0u;
    const unsigned msk1 = (n16 & 3) == 2 ? 0x0000ffffu : (n16 & 3) == 3 ? 0xffff0000u : 0u;
    float m_run = -INFINITY, l_run = 0.f;
    f32x4 olo = {0.f, 0.f, 0.f, 0.f}, ohi = {0.f, 0.f, 0.f, 0.f};
    while (!__all(GET_FLAG(4 + (lane & 3)))) __builtin_amdgcn_s_sleep(1);
    bf16x8 qf[4];
#pragma unroll
    for (int s = 0; s < 4; ++s) qf[s] = *reinterpret_cast<const bf16x8*>(q_s + ((n16 >> 2) * DH + s * 32 + g4 * 8) / 2);
    STAMP(2);
#define TILE(d) do { \
      f32x4 sc = {0.f, 0.f, 0.f, 0.f}; \
      _Pragma("unroll") for (int s = 0; s < 4; ++s) sc = __builtin_amdgcn_mfma_f32_16x16x32_bf16(__builtin_bit_cast(bf16x8, kb[d][s]), qf[s], sc, 0, 0, 0); \
      float tmax = fmaxf(fmaxf(sc[0], sc[1]), fmaxf(sc[2], sc[3])); \
      tmax = xrow_max(tmax); \
      const float mnew = fmaxf(m_run, tmax); \
      const float alpha = __builtin_amdgcn_exp2f((m_run - mnew) * SOFT_C); \
      const float mc = -mnew * SOFT_C; \
      float pr[4]; \
      _Pragma("unroll") for (int r = 0; r < 4; ++r) pr[r] = __builtin_amdgcn_exp2f(fmaf(sc[r], SOFT_C, mc)); \
      l_run = fmaf(l_run, alpha, (pr[0] + pr[1]) + (pr[2] + pr[3])); \
      m_run = mnew; \
      olo *= alpha; ohi *= alpha; \
      _Pragma("unroll") for (int r = 0; r < 4; ++r) { \
        const unsigned pk = cvtpk(pr[r], pr[r]); \
        const u32x2 bp = {pk & msk0, pk & msk1}; \
        const u32x2 vlo = {vb[d][r][0], vb[d][r][1]}, vhi = {vb[d][r][2], vb[d][r][3]}; \
        olo = __builtin_amdgcn_mfma_f32_16x16x16bf16_1k(__builtin_bit_cast(s16x4, vlo), __builtin_bit_cast(s16x4, bp), olo, 0, 0, 0); \
        ohi = __builtin_amdgcn_mfma_f32_16x16x16bf16_1k(__builtin_bit_cast(s16x4, vhi), __builtin_bit_cast(s16x4, bp), ohi, 0, 0, 0); \
      } \
      __builtin_amdgcn_sched_barrier(0); } while (0)
    if (streamer) {
#pragma unroll
      for (int d = 0; d < REM; ++d) { TILE(d); ISSUE(d, sbase + (unsigned)(DEPTH + d) * TSTEP); }
#pragma unroll 1
      for (int it = 0; it < NFULL - 1; ++it) {
        const unsigned snext = sbase + (unsigned)(REM + (it + 1) * DEPTH) * TSTEP;
#pragma unroll
        for (int d = 0; d < DEPTH; ++d) {
          TILE((d + REM) % DEPTH);
          ISSUE((d + REM) % DEPTH, snext + d * TSTEP);
        }
      }
#pragma unroll
      for (int d = 0; d < DEPTH; ++d) TILE((d + REM) % DEPTH);
    } else {
#pragma unroll
      for (int d = 0; d < HT; ++d) TILE(d);
    }
#undef TILE
#undef ISSUE
    STAMP(3);
    l_run = xrow_sum(l_run);
    float* mo = mrg_o + wave * 512 + (n16 >> 2) * DH + g4 * 32 + (n16 & 3);
#pragma unroll
    for (int r = 0; r < 4; ++r) { mo[r * 8] = olo[r]; mo[r * 8 + 4] = ohi[r]; }
    if (g4 == 0 && (n16 & 3) == 0) { mrg_m[wave * 4 + (n16 >> 2)] = m_run; mrg_l[wave * 4 + (n16 >> 2)] = l_run; }
  }
  const unsigned t40_lo = (unsigned)(tag << 24), t40_hi = (unsigned)(tag >> 8);
  asm volatile("" :: "v"(t40_lo), "v"(t40_hi));
  __syncthreads();
  {
    const int h = tid >> 7;
    float mw[8], M = -INFINITY;
#pragma unroll
    for (int v = 0; v < 8; ++v) { mw[v] = mrg_m[v * 4 + h]; M = fmaxf(M, mw[v]); }
    float o = 0.f, L = 0.f;
#pragma unroll
    for (int v = 0; v < 8; ++v) {
      const float f = __builtin_amdgcn_exp2f((mw[v] - M) * SOFT_C);
      o = fmaf(f, mrg_o[v * 512 + tid], o); L = fmaf(f, mrg_l[v * 4 + h], L);
    }
    __builtin_amdgcn_raw_buffer_store_b64(gr24(f24(o), t40_lo, t40_hi), wsr, WS_AP + (unsigned)(w * 512 + tid) * 8, 0, AUX_SC1);
    if ((tid & 127) < 2) {
      const unsigned mb = __float_as_uint(M);
      const unsigned p0 = (tid & 1) ? f24(L) : (mb >> 8), p1 = (tid & 1) ? 0u : (mb & 0xffu);
      STG((u32x4{p0 | t40_lo, t40_hi, p1 | t40_lo, t40_hi}), WS_ST + (unsigned)(((kvh * 4 + h) * 32 + bat * 16 + split) * 4 + 2 * (tid & 1)) * 8);
    }
  }
  STAMP(4);
  if (w < 64 && wave == 1) {
      const unsigned yoff = WS_YP + (unsigned)(frow * 1024 + w * 16 + fcc * 2) * 8;
      u32x4 yg[4];
      unsigned spins = 0;
      for (;;) {
        bool ok = true;
#pragma unroll
        for (int k = 0; k < 4; ++k) yg[k] = (k < 3 || lane < 32) ? LDG(yoff + k * 8 * 8192) : u32x4{t40_lo, t40_hi, t40_lo, t40_hi};
#pragma unroll
        for (int k = 0; k < 4; ++k) ok &= ok24x2(yg[k], t40_lo, t40_hi);
        if (__all(ok) || ++spins >= SPIN_CAP) break;
        asm volatile("" ::: "memory");
      }
      float y0 = (val24(yg[0][0]) + val24(yg[1][0])) + (val24(yg[2][0]) + val24(yg[3][0]));
      float y1 = (val24(yg[0][2]) + val24(yg[1][2])) + (val24(yg[2][2]) + val24(yg[3][2]));
      y0 += dpp<ROR8>(y0); y0 = rbf(xrow_sum(y0));
      y1 += dpp<ROR8>(y1); y1 = rbf(xrow_sum(y1));
      if (lane < 8) *reinterpret_cast<f32x2*>(g_s + lane * 2) = f32x2{rbf(y0 / (1.f + __expf(-y0))), rbf(y1 / (1.f + __expf(-y1)))};
    }
  bf16x8 wof[2];
  {
    const unsigned short* ws16 = reinterpret_cast<const unsigned short*>(wo_s) + wave * 16 + n16;
#pragma unroll
    for (int s = 0; s < 2; ++s) {
      u32x4 t;
#pragma unroll
      for (int j = 0; j < 4; ++j)
        t[j] = (unsigned)ws16[(s * 32 + g4 * 8 + 2 * j) * (17 * 8)] | ((unsigned)ws16[(s * 32 + g4 * 8 + 2 * j + 1) * (17 * 8)] << 16);
      wof[s] = __builtin_bit_cast(bf16x8, t);
    }
  }
  {
    const unsigned o2 = WS_ST + (unsigned)((kvh * 4 + (idx >> 3)) * 128 + lane * 2) * 8;
    const unsigned o1 = WS_AP + (unsigned)((kvh + 8 * (lane >> 5) + 16 * ((lane >> 1) & 15)) * 512 + (idx >> 2) * 64 + wave * 8 + (lane & 1) * 4) * 8;
    u32x4 gs, ga, gb;
    unsigned spins = 0;
    if (wave == 0) {
      gs = LDG(o2);
      for (;;) {
        const bool okl = ok24x2(gs, t40_lo, t40_hi);
        if (__popcll(__ballot(!okl)) <= 2 * EARLY_GO || ++spins >= SPIN_CAP) break;
        __builtin_amdgcn_s_sleep(POLL_SLEEP);
        asm volatile("" ::: "memory");
        if (!okl) gs = LDG(o2);
      }
      SET_FLAG(8);
    } else {
      while (!GET_FLAG(8)) __builtin_amdgcn_s_sleep(0);
    }
    gs = LDG(o2); ga = LDG(o1); gb = LDG(o1 + 16);
    for (;;) {
      const bool okl = ok24x2(gs, t40_lo, t40_hi) & ok24x2(ga, t40_lo, t40_hi) & ok24x2(gb, t40_lo, t40_hi);
      if (__all(okl) || ++spins >= SPIN_CAP) break;
      asm volatile("" ::: "memory");
      if (!okl) { gs = LDG(o2); ga = LDG(o1); gb = LDG(o1 + 16); }
    }
    const float mine = __uint_as_float((gs[0] << 8) | ((lane & 1) ? 0u : (gs[2] & 0xffu)));
    const float other = dpp<XOR1>(mine);
    const float m = (lane & 1) ? other : mine, l = (lane & 1) ? mine : other;
    float M = m; M = fmaxf(M, dpp<XOR2>(M)); M = fmaxf(M, dpp<XOR7>(M)); M = fmaxf(M, dpp<ROR8>(M));
    { auto t = __builtin_amdgcn_permlane16_swap(__float_as_uint(M), __float_as_uint(M), false, false); M = fmaxf(__uint_as_float(t[0]), __uint_as_float(t[1])); }
    const float f = __builtin_amdgcn_exp2f((m - M) * SOFT_C);
    float L = f * l, a0 = f * val24(ga[0]), a1 = f * val24(ga[2]), a2 = f * val24(gb[0]), a3 = f * val24(gb[2]);
    a0 = tr16(a0, a2); a1 = tr16(a1, a3); L = tr16(L, L);
    a0 = tr8(a0, a1, (lane >> 3) & 1); L += dpp<ROR8>(L);
    a0 += dpp<XOR2>(a0); L += dpp<XOR2>(L);
    { const float up = dpp<ROR4>(a0), dn = dpp<ROR12>(a0); a0 += ((lane >> 2) & 1) ? up : dn; }
    L += dpp<XOR7>(L);
    const float o = a0 / L;
    if ((lane & 6) == 0) o_s[(lane >> 5) * 64 + wave * 8 + (lane & 1) * 4 + ((lane >> 4) & 1) * 2 + ((lane >> 3) & 1)] = (unsigned short)(cvtpk(o, o) & 0xffffu);
  }
  __syncthreads();
  STAMP(5);
  {
    f32x4 d = {0.f, 0.f, 0.f, 0.f};
#pragma unroll
    for (int s = 0; s < 2; ++s) {
      u32x4 av = *reinterpret_cast<const u32x4*>(o_s + (n16 & 1) * 64 + s * 32 + g4 * 8);
      if (n16 >= 2) av = u32x4{0u, 0u, 0u, 0u};
      d = __builtin_amdgcn_mfma_f32_16x16x32_bf16(__builtin_bit_cast(bf16x8, av), wof[s], d, 0, 0, 0);
    }
    if (lane < 16) {
      const unsigned so = WS_SP + (unsigned)((kvh * 8 + (idx >> 2)) * 1024 + ((idx & 3) * 8 + wave) * 32 + lane) * 8;
      __builtin_amdgcn_raw_buffer_store_b64(gr24(f24(d[0]), t40_lo, t40_hi), wsr, so, 0, AUX_SC1);
      __builtin_amdgcn_raw_buffer_store_b64(gr24(f24(d[1]), t40_lo, t40_hi), wsr, so + 128, 0, AUX_SC1);
    }
  }
  STAMP(6);
  if (w < 64) {
    const unsigned soff = WS_SP + (unsigned)((wave * 8 + frow) * 1024 + w * 16 + fcc * 2) * 8;
    const int oidx = (w & 1) * DM + (w >> 1) * 16 + fcc * 2;
    const unsigned xr = *reinterpret_cast<const unsigned*>(p.x + oidx);
    u32x4 sg;
    unsigned spins = 0;
    sg = LDG(soff);
    for (;;) {
      const bool okl = ok24x2(sg, t40_lo, t40_hi);
      if (__all(okl) || ++spins >= SPIN_CAP) break;
      asm volatile("" ::: "memory");
      if (!okl) sg = LDG(soff);
    }
    float s0 = val24(sg[0]), s1 = val24(sg[2]);
    s0 += dpp<ROR8>(s0); s0 = xrow_sum(s0);
    s1 += dpp<ROR8>(s1); s1 = xrow_sum(s1);
    if (lane < 8) *reinterpret_cast<f32x2*>(fin_s + wave * 16 + lane * 2) = f32x2{s0, s1};
    __syncthreads();
    if (wave == 0 && lane < 8) {
      float sa = 0.f, sb = 0.f;
#pragma unroll
      for (int v = 0; v < 8; ++v) { const f32x2 t = *reinterpret_cast<const f32x2*>(fin_s + v * 16 + lane * 2); sa += t[0]; sb += t[1]; }
      const f32x2 g = *reinterpret_cast<const f32x2*>(g_s + lane * 2);
      const float r0 = bf_lo(xr) + rbf(rbf(sa) * g[0]), r1 = bf_hi(xr) + rbf(rbf(sb) * g[1]);
      *reinterpret_cast<unsigned*>(p.out + oidx) = cvtpk(r0, r1);
      if (w == 0 && lane == 0) __hip_atomic_store(reinterpret_cast<u64*>(p.ws + WS_EPOCH), tag, __ATOMIC_RELAXED, __HIP_MEMORY_SCOPE_AGENT);
    }
  }
  STAMP(7);
}

}

extern "C" void kernel_launch(void* const* d_in, const int* in_sizes, int n_in, void* d_out, int out_size, void* d_ws, size_t ws_size,
                              hipStream_t stream) {
  Params p;
  p.x = (const unsigned short*)d_in[0];
  p.k = (const unsigned short*)d_in[1];
  p.v = (const unsigned short*)d_in[2];
  p.wq = (const unsigned short*)d_in[3];
  p.wo = (const unsigned short*)d_in[4];
  p.w1 = (const unsigned short*)d_in[6];
  p.w2 = (const unsigned short*)d_in[7];
  p.out = (unsigned short*)d_out;
  p.ws = (unsigned char*)d_ws;
  hipLaunchKernelGGL(fused_decode_kernel, dim3(NWG), dim3(NTHR), 0, stream, p);
}
```

```cpp
#define THR 49.f
#define REPORT 7
#include <hip/hip_runtime.h>
#include <hip/hip_bf16.h>
#include <stdint.h>

typedef unsigned u32x4 __attribute__((ext_vector_type(4)));
typedef unsigned u32x2 __attribute__((ext_vector_type(2)));
typedef float f32x4 __attribute__((ext_vector_type(4)));
typedef float f32x2 __attribute__((ext_vector_type(2)));
typedef __bf16 bf16x2 __attribute__((ext_vector_type(2)));
typedef __bf16 bf16x8 __attribute__((ext_vector_type(8)));
typedef short s16x4 __attribute__((ext_vector_type(4)));
typedef unsigned long long u64;

namespace {

constexpr int NWG = 256, NTHR = 512, NWAVE = 8;
constexpr int SK = 32768, DH = 128, DM = 512, DFF = 1792, NQ = 4096;
#define DEPTH_ 6
constexpr int DEPTH = DEPTH_;
#define HT DEPTH
constexpr int NFULL = (32 - HT) / DEPTH, REM = (32 - HT) % DEPTH;
static_assert(4 * (NFULL * DEPTH + REM) + 4 * HT == 128, "128 tiles of 16 keys per workgroup");
constexpr int NFF = 224;
constexpr unsigned WS_EPOCH = 0;
constexpr unsigned WS_QG = 4096;
constexpr unsigned WS_HP = WS_QG + 4 * 2 * NQ * 8;
constexpr unsigned WS_AP = WS_HP + 8 * 28 * 128 * 8;
constexpr unsigned WS_ST = WS_AP + NWG * 4 * 128 * 8;
constexpr unsigned WS_YP = WS_ST + 8 * 4 * 32 * 4 * 8;
constexpr unsigned WS_SP = WS_YP + 28 * 1024 * 8;
constexpr unsigned WS_END = WS_SP + 64 * 1024 * 8;
constexpr unsigned WS_DBG = 8u << 20;
constexpr unsigned SPIN_CAP = 1u << 16;
constexpr int AUX_NT = 2, AUX_SC1 = 16;
constexpr float SOFT_C = 0.08838834764831845f * 1.4426950408889634f;

__device__ __forceinline__ float bf_lo(unsigned u) { return __uint_as_float(u << 16); }
__device__ __forceinline__ float bf_hi(unsigned u) { return __uint_as_float(u & 0xffff0000u); }
__device__ __forceinline__ unsigned cvtpk(float lo, float hi) {
  return __builtin_bit_cast(unsigned, __builtin_convertvector(f32x2{lo, hi}, bf16x2));
}
__device__ __forceinline__ float rbf(float x) { return bf_lo(cvtpk(x, x)); }
template <int CTRL> __device__ __forceinline__ float dpp(float x) {
  return __builtin_bit_cast(float, __builtin_amdgcn_mov_dpp(__builtin_bit_cast(int, x), CTRL, 0xf, 0xf, true));
}
constexpr int ROR4 = 0x124, ROR8 = 0x128, ROR12 = 0x12C, XOR1 = 0xB1, XOR2 = 0x4E, XOR7 = 0x141;
__device__ __forceinline__ float xrow_max(float x) {
  auto s = __builtin_amdgcn_permlane16_swap(__float_as_uint(x), __float_as_uint(x), false, false);
  x = fmaxf(__uint_as_float(s[0]), __uint_as_float(s[1]));
  auto t = __builtin_amdgcn_permlane32_swap(__float_as_uint(x), __float_as_uint(x), false, false);
  return fmaxf(__uint_as_float(t[0]), __uint_as_float(t[1]));
}
__device__ __forceinline__ float xrow_sum(float x) {
  auto s = __builtin_amdgcn_permlane16_swap(__float_as_uint(x), __float_as_uint(x), false, false);
  x = __uint_as_float(s[0]) + __uint_as_float(s[1]);
  auto t = __builtin_amdgcn_permlane32_swap(__float_as_uint(x), __float_as_uint(x), false, false);
  return __uint_as_float(t[0]) + __uint_as_float(t[1]);
}
__device__ __forceinline__ float wave_sum(float x) {
  x += dpp<XOR1>(x); x += dpp<XOR2>(x); x += dpp<XOR7>(x); x += dpp<ROR8>(x);
  return xrow_sum(x);
}
__device__ __forceinline__ float tr32(float a, float b) {
  auto t = __builtin_amdgcn_permlane32_swap(__float_as_uint(a), __float_as_uint(b), false, false);
  return __uint_as_float(t[0]) + __uint_as_float(t[1]);
}
__device__ __forceinline__ float tr16(float a, float b) {
  auto t = __builtin_amdgcn_permlane16_swap(__float_as_uint(a), __float_as_uint(b), false, false);
  return __uint_as_float(t[0]) + __uint_as_float(t[1]);
}
__device__ __forceinline__ float tr8(float a, float b, bool bit3) {
  const float keep = bit3 ? b : a, send = bit3 ? a : b;
  return keep + dpp<ROR8>(send);
}
__device__ __forceinline__ unsigned f24(float x) { return (__float_as_uint(x) + 0x80u) >> 8; }
__device__ __forceinline__ u32x2 gr24(unsigned payload24, unsigned tlo8, unsigned thi) { return u32x2{payload24 | tlo8, thi}; }
__device__ __forceinline__ bool ok24(unsigned lo, unsigned hi, unsigned tlo8, unsigned thi) { return ((lo & 0xff000000u) == tlo8) & (hi == thi); }
__device__ __forceinline__ float val24(unsigned lo) { return __uint_as_float(lo << 8); }

struct Params {
  const unsigned short* x; const unsigned short* k; const unsigned short* v; const unsigned short* wq; const unsigned short* wo;
  const unsigned short* w1; const unsigned short* w2; unsigned short* out; unsigned char* ws;
};

#define STAMP(i) do { stv[i] = __builtin_amdgcn_s_memrealtime(); } while (0)
#define Q_DELAY 32
#define POLL_SLEEP 16
#define EARLY_GO 3
#define PRE_BAR 2
#define LDNT(p_) __builtin_nontemporal_load(reinterpret_cast<const u32x4*>(p_))

__device__ __forceinline__ void layernorm_to_lds(const u32x4 xa, const u32x4 xb, float* dst, int lane) {
  float e0[8], e1[8];
#pragma unroll
  for (int j = 0; j < 4; ++j) { e0[2 * j] = bf_lo(xa[j]); e0[2 * j + 1] = bf_hi(xa[j]); e1[2 * j] = bf_lo(xb[j]); e1[2 * j + 1] = bf_hi(xb[j]); }
  float s0 = 0.f, s1 = 0.f;
#pragma unroll
  for (int j = 0; j < 8; ++j) { s0 += e0[j]; s1 += e1[j]; }
  s0 = wave_sum(s0) * (1.f / DM); s1 = wave_sum(s1) * (1.f / DM);
  float v0 = 0.f, v1 = 0.f;
#pragma unroll
  for (int j = 0; j < 8; ++j) { const float a = e0[j] - s0, b = e1[j] - s1; v0 = fmaf(a, a, v0); v1 = fmaf(b, b, v1); }
  v0 = wave_sum(v0) * (1.f / DM); v1 = wave_sum(v1) * (1.f / DM);
  const float m0 = rbf(s0), m1 = rbf(s1);
  const float d0 = 1.f / rbf(sqrtf(rbf(rbf(v0) + 1e-5f))), d1 = 1.f / rbf(sqrtf(rbf(rbf(v1) + 1e-5f)));
#pragma unroll
  for (int j = 0; j < 8; j += 2)
    *reinterpret_cast<f32x4*>(dst + (8 * lane + j) * 2) =
        f32x4{rbf(rbf(e0[j] - m0) * d0), rbf(rbf(e1[j] - m1) * d1), rbf(rbf(e0[j + 1] - m0) * d0), rbf(rbf(e1[j + 1] - m1) * d1)};
}

__device__ __forceinline__ bool ok24x2(const u32x4 g, unsigned tlo8, unsigned thi) { return ok24(g[0], g[1], tlo8, thi) & ok24(g[2], g[3], tlo8, thi); }
__device__ __forceinline__ u32x4 gr24x2(float a, float b, unsigned tlo8, unsigned thi) { return u32x4{f24(a) | tlo8, thi, f24(b) | tlo8, thi}; }

__global__ void __launch_bounds__(NTHR, 2) fused_decode_kernel(Params p) {
  __shared__ __attribute__((aligned(16))) float lds[13312];
  float* xn_s = lds;
  float* red_q = lds + 1024;
  float* mrg_m = lds + 1536;
  float* mrg_l = lds + 1568;
  unsigned short* o_s = reinterpret_cast<unsigned short*>(lds + 2560);
  float* fin_s = lds + 1632;
  float* tr_h = lds + 1760;
  float* h_s = lds + 1888;
  float* g_s = lds + 2016;
  unsigned* q_s = reinterpret_cast<unsigned*>(lds + 2048);
  unsigned* flag_w = reinterpret_cast<unsigned*>(lds + 2304);
  float* mrg_o = lds + 4608;
  u32x4* wo_s = reinterpret_cast<u32x4*>(lds + 8704);

  const int w = blockIdx.x, tid = threadIdx.x, lane = tid & 63;
  const int wave = __builtin_amdgcn_readfirstlane(tid >> 6);
  const int kvh = w & 7, bat = (w >> 3) & 1, split = w >> 4, idx = w >> 3;
  const int n16 = lane & 15, g4 = lane >> 4, frow = lane >> 3, fcc = lane & 7;
  u64 stv[16];
#pragma unroll
  for (int i = 0; i < 16; ++i) stv[i] = 0;
  stv[14] = __builtin_amdgcn_s_memtime();
  STAMP(0);

  const __amdgpu_buffer_rsrc_t wsr = __builtin_amdgcn_make_buffer_rsrc((void*)p.ws, 0, WS_END, 0x00020000);
#define LOAD_EPOCH() __hip_atomic_load(reinterpret_cast<const u64*>(p.ws + WS_EPOCH), __ATOMIC_RELAXED, __HIP_MEMORY_SCOPE_AGENT)
#define WG_BARRIER() do { __builtin_amdgcn_sched_barrier(0); asm volatile("s_waitcnt lgkmcnt(0)\n\ts_barrier" ::: "memory"); __builtin_amdgcn_sched_barrier(0); } while (0)
#define LDG(off_) __builtin_amdgcn_raw_buffer_load_b128(wsr, (off_), 0, AUX_SC1)
#define STG(v_, off_) __builtin_amdgcn_raw_buffer_store_b128((v_), wsr, (off_), 0, AUX_SC1)
  const bool streamer = wave >= 4;
  const int f2kb = (w & 7) + 8 * (w >> 6), f2cb = (w >> 3) & 7;

#define SET_FLAG(i_) __hip_atomic_store(&flag_w[i_], 1u, __ATOMIC_RELEASE, __HIP_MEMORY_SCOPE_WORKGROUP)
#define GET_FLAG(i_) (__hip_atomic_load(&flag_w[i_], __ATOMIC_ACQUIRE, __HIP_MEMORY_SCOPE_WORKGROUP) != 0u)
  const __amdgpu_buffer_rsrc_t kr = __builtin_amdgcn_make_buffer_rsrc((void*)p.k, 0, 2u * 8 * SK * DH * 2, 0x00020000);
  const __amdgpu_buffer_rsrc_t vr = __builtin_amdgcn_make_buffer_rsrc((void*)p.v, 0, 2u * 8 * SK * DH * 2, 0x00020000);
  const unsigned kvoff = (unsigned)(((n16 & 3) * 4 + (n16 >> 2)) * 256 + g4 * 16);
  const unsigned vvoff = (unsigned)lane * 16;
  const unsigned sbase = (unsigned)((bat * 8 + kvh) * SK + split * 2048 + ((streamer ? 4 * HT : 0) + (wave & 3)) * 16) * (DH * 2);
  constexpr unsigned TSTEP = 4 * 16 * DH * 2;
  u32x4 kb[DEPTH][4], vb[DEPTH][4];
#define ISSUE(d_, so_) do { \
    _Pragma("unroll") for (int s_ = 0; s_ < 4; ++s_) kb[d_][s_] = __builtin_amdgcn_raw_buffer_load_b128(kr, kvoff + s_ * 64, (so_), AUX_NT); \
    _Pragma("unroll") for (int s_ = 0; s_ < 4; ++s_) vb[d_][s_] = __builtin_amdgcn_raw_buffer_load_b128(vr, vvoff + s_ * 1024, (so_), AUX_NT); \
    __builtin_amdgcn_sched_barrier(0); } while (0)
  u64 tag;
  if (__builtin_expect(streamer, 1)) {
    STAMP(9);
#pragma unroll
    for (int d = 0; d < PRE_BAR; ++d) ISSUE(d, sbase + d * TSTEP);
    WG_BARRIER();
#pragma unroll
    for (int d = PRE_BAR; d < DEPTH; ++d) ISSUE(d, sbase + d * TSTEP);
    tag = LOAD_EPOCH() + 1;
  } else {
    const u32x4 xa = *reinterpret_cast<const u32x4*>(p.x + 8 * lane);
    const u32x4 xb = *reinterpret_cast<const u32x4*>(p.x + DM + 8 * lane);
    tag = LOAD_EPOCH() + 1;
    __builtin_amdgcn_sched_barrier(0);
    const int qrow = (w >> 6) * 128 + wave * 32 + frow;
    const unsigned short* wqp = p.wq + (size_t)qrow * NQ + (w & 63) * 64 + fcc * 8;
    u32x4 wv[4];
#pragma unroll
    for (int k = 0; k < 4; ++k) wv[k] = LDNT(wqp + (size_t)k * 8 * NQ);
    __builtin_amdgcn_sched_barrier(0);
    const bool has_ff = w < NFF;
    const int f1row = (w / 28) * 64 + frow;
    u32x4 wt[16];
    if (wave == 1) {
      if (has_ff) {
#pragma unroll
        for (int k = 0; k < 8; ++k) wt[k] = LDNT(p.w1 + (size_t)(f1row + 8 * k) * DFF + (w % 28) * 64 + fcc * 8);
      }
    } else if (wave == 2) {
#pragma unroll
      for (int k = 0; k < 16; ++k) wt[k] = LDNT(p.wo + (size_t)(kvh * 512 + (idx >> 2) * 64 + k * 4 + g4) * DM + (idx & 3) * 128 + n16 * 8);
    } else if (wave == 3) {
      if (f2kb < 28) {
#pragma unroll
        for (int k = 0; k < 8; ++k) wt[k] = LDNT(p.w2 + (size_t)(f2kb * 64 + frow + 8 * k) * DM + f2cb * 64 + fcc * 8);
      }
    }
    if (tid < 16) flag_w[tid] = 0u;
    WG_BARRIER();
    layernorm_to_lds(xa, xb, xn_s, lane);
    const unsigned t40_lo = (unsigned)(tag << 24), t40_hi = (unsigned)(tag >> 8);
    const bool b3 = (lane >> 3) & 1;
    {
      float qa[16];
#pragma unroll
      for (int j = 0; j < 16; ++j) qa[j] = 0.f;
#pragma unroll
      for (int k = 0; k < 4; ++k) {
        const f32x2 xv = *reinterpret_cast<const f32x2*>(xn_s + (qrow + 8 * k) * 2);
#pragma unroll
        for (int j = 0; j < 4; ++j) {
          const float l0 = bf_lo(wv[k][j]), h0 = bf_hi(wv[k][j]);
          qa[2 * j] = fmaf(xv[0], l0, qa[2 * j]); qa[2 * j + 1] = fmaf(xv[0], h0, qa[2 * j + 1]);
          qa[8 + 2 * j] = fmaf(xv[1], l0, qa[8 + 2 * j]); qa[8 + 2 * j + 1] = fmaf(xv[1], h0, qa[8 + 2 * j + 1]);
        }
      }
#pragma unroll
      for (int j = 0; j < 8; ++j) qa[j] = tr32(qa[j], qa[j + 8]);
#pragma unroll
      for (int j = 0; j < 4; ++j) qa[j] = tr16(qa[j], qa[j + 4]);
      *reinterpret_cast<f32x2*>(red_q + wave * 128 + (lane >> 5) * 64 + fcc * 8 + ((lane >> 3) & 3) * 2) = f32x2{tr8(qa[0], qa[2], b3), tr8(qa[1], qa[3], b3)};
      SET_FLAG(wave);
    }
    if (wave == 0) {
      while (!__all(GET_FLAG(lane & 3))) __builtin_amdgcn_s_sleep(0);
      f32x2 t = *reinterpret_cast<const f32x2*>(red_q + 2 * lane);
#pragma unroll
      for (int v = 1; v < 4; ++v) t += *reinterpret_cast<const f32x2*>(red_q + v * 128 + 2 * lane);
      STG(gr24x2(t[0], t[1], t40_lo, t40_hi), WS_QG + (unsigned)(((w >> 6) * 2 + (lane >> 5)) * NQ + (w & 63) * 64 + (lane & 31) * 2) * 8);
      STAMP(1);
    } else if (wave == 1) {
      if (has_ff) {
        float ha[16];
#pragma unroll
        for (int j = 0; j < 16; ++j) ha[j] = 0.f;
#pragma unroll
        for (int k = 0; k < 8; ++k) {
          const f32x2 xv = *reinterpret_cast<const f32x2*>(xn_s + (f1row + 8 * k) * 2);
#pragma unroll
          for (int j = 0; j < 4; ++j) {
            const float l0 = bf_lo(wt[k][j]), h0 = bf_hi(wt[k][j]);
            ha[2 * j] = fmaf(xv[0], l0, ha[2 * j]); ha[2 * j + 1] = fmaf(xv[0], h0, ha[2 * j + 1]);
            ha[8 + 2 * j] = fmaf(xv[1], l0, ha[8 + 2 * j]); ha[8 + 2 * j + 1] = fmaf(xv[1], h0, ha[8 + 2 * j + 1]);
          }
        }
#pragma unroll
        for (int j = 0; j < 8; ++j) ha[j] = tr32(ha[j], ha[j + 8]);
#pragma unroll
        for (int j = 0; j < 4; ++j) ha[j] = tr16(ha[j], ha[j + 4]);
        { const int u = fcc * 8 + (frow & 3) * 2;
          tr_h[u * 2 + (lane >> 5)] = tr8(ha[0], ha[2], b3); tr_h[u * 2 + 2 + (lane >> 5)] = tr8(ha[1], ha[3], b3); }
        const f32x2 t = *reinterpret_cast<const f32x2*>(tr_h + 2 * lane);
        STG(gr24x2(t[0], t[1], t40_lo, t40_hi), WS_HP + (unsigned)(w * 128 + 2 * lane) * 8);
        STAMP(10);
      }
    } else if (wave == 2) {
#pragma unroll
      for (int k = 0; k < 16; ++k) wo_s[(k * 4 + g4) * 17 + n16] = wt[k];
    }
    __builtin_amdgcn_sched_barrier(0);
    {
      const unsigned qoff = WS_QG + (unsigned)(bat * NQ + kvh * 512 + wave * 128 + 2 * lane) * 8;
      u32x4 qg[4];
      unsigned spins = 0;
      __builtin_amdgcn_s_sleep(Q_DELAY);
#pragma unroll
      for (int k = 0; k < 4; ++k) qg[k] = LDG(qoff + k * (2 * NQ * 8));
      for (;;) {
        const bool k0 = __all(ok24x2(qg[0], t40_lo, t40_hi)), k1 = __all(ok24x2(qg[1], t40_lo, t40_hi));
        const bool k2 = __all(ok24x2(qg[2], t40_lo, t40_hi)), k3 = __all(ok24x2(qg[3], t40_lo, t40_hi));
        if ((k0 & k1 & k2 & k3) || ++spins >= SPIN_CAP) break;
        __builtin_amdgcn_s_sleep(8);
        asm volatile("" ::: "memory");
        if (!k0) qg[0] = LDG(qoff);
        if (!k1) qg[1] = LDG(qoff + 1 * (2 * NQ * 8));
        if (!k2) qg[2] = LDG(qoff + 2 * (2 * NQ * 8));
        if (!k3) qg[3] = LDG(qoff + 3 * (2 * NQ * 8));
      }
      q_s[wave * 64 + lane] = cvtpk((val24(qg[0][0]) + val24(qg[1][0])) + (val24(qg[2][0]) + val24(qg[3][0])),
                                    (val24(qg[0][2]) + val24(qg[1][2])) + (val24(qg[2][2]) + val24(qg[3][2])));
      SET_FLAG(4 + wave);
      STAMP(11);
    }
    if (wave == 3) {
    if (f2kb < 28) {
      const unsigned hoff = WS_HP + (unsigned)(f2kb * 128 + 2 * lane) * 8;
      u32x4 hg[8];
      unsigned spins = 0;
#pragma unroll
      for (int k = 0; k < 8; ++k) hg[k] = LDG(hoff + k * (28 * 128 * 8));
      for (;;) {
        bool ok = true;
#pragma unroll
        for (int k = 0; k < 8; ++k) ok &= ok24x2(hg[k], t40_lo, t40_hi);
        if (__all(ok) || ++spins >= SPIN_CAP) break;
        __builtin_amdgcn_s_sleep(8);
        asm volatile("" ::: "memory");
#pragma unroll
        for (int k = 0; k < 8; ++k) hg[k] = LDG(hoff + k * (28 * 128 * 8));
      }
      float h0 = 0.f, h1 = 0.f;
#pragma unroll
      for (int k = 0; k < 8; ++k) { h0 += val24(hg[k][0]); h1 += val24(hg[k][2]); }
      *reinterpret_cast<f32x2*>(h_s + 2 * lane) = f32x2{fmaxf(rbf(h0), 0.f), fmaxf(rbf(h1), 0.f)};
      float ya[16];
#pragma unroll
      for (int j = 0; j < 16; ++j) ya[j] = 0.f;
#pragma unroll
      for (int k = 0; k < 8; ++k) {
        const f32x2 hv = *reinterpret_cast<const f32x2*>(h_s + (frow + 8 * k) * 2);
#pragma unroll
        for (int j = 0; j < 4; ++j) {
          const float l0 = bf_lo(wt[k][j]), hh = bf_hi(wt[k][j]);
          ya[2 * j] = fmaf(hv[0], l0, ya[2 * j]); ya[2 * j + 1] = fmaf(hv[0], hh, ya[2 * j + 1]);
          ya[8 + 2 * j] = fmaf(hv[1], l0, ya[8 + 2 * j]); ya[8 + 2 * j + 1] = fmaf(hv[1], hh, ya[8 + 2 * j + 1]);
        }
      }
#pragma unroll
      for (int j = 0; j < 8; ++j) ya[j] = tr32(ya[j], ya[j + 8]);
#pragma unroll
      for (int j = 0; j < 4; ++j) ya[j] = tr16(ya[j], ya[j + 4]);
      asm volatile("" ::: "memory");
      *reinterpret_cast<f32x2*>(h_s + ((fcc >> 1) * 2 + (lane >> 5)) * 16 + (fcc & 1) * 8 + (frow & 3) * 2) = f32x2{tr8(ya[0], ya[2], b3), tr8(ya[1], ya[3], b3)};
      const f32x2 t = *reinterpret_cast<const f32x2*>(h_s + 2 * lane);
      STG(gr24x2(t[0], t[1], t40_lo, t40_hi), WS_YP + (unsigned)(f2kb * 1024 + f2cb * 128 + 2 * lane) * 8);
    }
      STAMP(8);
    }
    __builtin_amdgcn_sched_barrier(0);
#pragma unroll
    for (int d = 0; d < HT; ++d) ISSUE(d, sbase + d * TSTEP);
  }

  {
    const unsigned msk0 = (n16 & 3) == 0 ? 0x0000ffffu : (n16 & 3) == 1 ? 0xffff0000u : 0u;
    const unsigned msk1 = (n16 & 3) == 2 ? 0x0000ffffu : (n16 & 3) == 3 ? 0xffff0000u : 0u;
    float m_run = -INFINITY, l_run = 0.f;
    f32x4 olo = {0.f, 0.f, 0.f, 0.f}, ohi = {0.f, 0.f, 0.f, 0.f};
    while (!__all(GET_FLAG(4 + (lane & 3)))) __builtin_amdgcn_s_sleep(1);
    bf16x8 qf[4];
#pragma unroll
    for (int s = 0; s < 4; ++s) qf[s] = *reinterpret_cast<const bf16x8*>(q_s + ((n16 >> 2) * DH + s * 32 + g4 * 8) / 2);
    STAMP(2);
#define TILE(d) do { \
      f32x4 sc = {0.f, 0.f, 0.f, 0.f}; \
      _Pragma("unroll") for (int s = 0; s < 4; ++s) sc = __builtin_amdgcn_mfma_f32_16x16x32_bf16(__builtin_bit_cast(bf16x8, kb[d][s]), qf[s], sc, 0, 0, 0); \
      float tmax = fmaxf(fmaxf(sc[0], sc[1]), fmaxf(sc[2], sc[3])); \
      tmax = xrow_max(tmax); \
      const float mnew = fmaxf(m_run, tmax); \
      const float alpha = __builtin_amdgcn_exp2f((m_run - mnew) * SOFT_C); \
      const float mc = -mnew * SOFT_C; \
      float pr[4]; \
      _Pragma("unroll") for (int r = 0; r < 4; ++r) pr[r] = __builtin_amdgcn_exp2f(fmaf(sc[r], SOFT_C, mc)); \
      l_run = fmaf(l_run, alpha, (pr[0] + pr[1]) + (pr[2] + pr[3])); \
      m_run = mnew; \
      olo *= alpha; ohi *= alpha; \
      _Pragma("unroll") for (int r = 0; r < 4; ++r) { \
        const unsigned pk = cvtpk(pr[r], pr[r]); \
        const u32x2 bp = {pk & msk0, pk & msk1}; \
        const u32x2 vlo = {vb[d][r][0], vb[d][r][1]}, vhi = {vb[d][r][2], vb[d][r][3]}; \
        olo = __builtin_amdgcn_mfma_f32_16x16x16bf16_1k(__builtin_bit_cast(s16x4, vlo), __builtin_bit_cast(s16x4, bp), olo, 0, 0, 0); \
        ohi = __builtin_amdgcn_mfma_f32_16x16x16bf16_1k(__builtin_bit_cast(s16x4, vhi), __builtin_bit_cast(s16x4, bp), ohi, 0, 0, 0); \
      } \
      __builtin_amdgcn_sched_barrier(0); } while (0)
    if (streamer) {
#pragma unroll
      for (int d = 0; d < REM; ++d) { TILE(d); ISSUE(d, sbase + (unsigned)(DEPTH + d) * TSTEP); }
#pragma unroll 1
      for (int it = 0; it < NFULL - 1; ++it) {
        const unsigned snext = sbase + (unsigned)(REM + (it + 1) * DEPTH) * TSTEP;
#pragma unroll
        for (int d = 0; d < DEPTH; ++d) {
          TILE((d + REM) % DEPTH);
          ISSUE((d + REM) % DEPTH, snext + d * TSTEP);
        }
      }
#pragma unroll
      for (int d = 0; d < DEPTH; ++d) TILE((d + REM) % DEPTH);
    } else {
#pragma unroll
      for (int d = 0; d < HT; ++d) TILE(d);
    }
#undef TILE
#undef ISSUE
    STAMP(3);
    l_run = xrow_sum(l_run);
    float* mo = mrg_o + wave * 512 + (n16 >> 2) * DH + g4 * 32 + (n16 & 3);
#pragma unroll
    for (int r = 0; r < 4; ++r) { mo[r * 8] = olo[r]; mo[r * 8 + 4] = ohi[r]; }
    if (g4 == 0 && (n16 & 3) == 0) { mrg_m[wave * 4 + (n16 >> 2)] = m_run; mrg_l[wave * 4 + (n16 >> 2)] = l_run; }
  }
  const unsigned t40_lo = (unsigned)(tag << 24), t40_hi = (unsigned)(tag >> 8);
  asm volatile("" :: "v"(t40_lo), "v"(t40_hi));
  __syncthreads();
  {
    const int h = tid >> 7;
    float mw[8], M = -INFINITY;
#pragma unroll
    for (int v = 0; v < 8; ++v) { mw[v] = mrg_m[v * 4 + h]; M = fmaxf(M, mw[v]); }
    float o = 0.f, L = 0.f;
#pragma unroll
    for (int v = 0; v < 8; ++v) {
      const float f = __builtin_amdgcn_exp2f((mw[v] - M) * SOFT_C);
      o = fmaf(f, mrg_o[v * 512 + tid], o); L = fmaf(f, mrg_l[v * 4 + h], L);
    }
    __builtin_amdgcn_raw_buffer_store_b64(gr24(f24(o), t40_lo, t40_hi), wsr, WS_AP + (unsigned)(w * 512 + tid) * 8, 0, AUX_SC1);
    if ((tid & 127) < 2) {
      const unsigned mb = __float_as_uint(M);
      const unsigned p0 = (tid & 1) ? f24(L) : (mb >> 8), p1 = (tid & 1) ? 0u : (mb & 0xffu);
      STG((u32x4{p0 | t40_lo, t40_hi, p1 | t40_lo, t40_hi}), WS_ST + (unsigned)(((kvh * 4 + h) * 32 + bat * 16 + split) * 4 + 2 * (tid & 1)) * 8);
    }
  }
  STAMP(4);
  if (w < 64 && wave == 1) {
      const unsigned yoff = WS_YP + (unsigned)(frow * 1024 + w * 16 + fcc * 2) * 8;
      u32x4 yg[4];
      unsigned spins = 0;
      for (;;) {
        bool ok = true;
#pragma unroll
        for (int k = 0; k < 4; ++k) yg[k] = (k < 3 || lane < 32) ? LDG(yoff + k * 8 * 8192) : u32x4{t40_lo, t40_hi, t40_lo, t40_hi};
#pragma unroll
        for (int k = 0; k < 4; ++k) ok &= ok24x2(yg[k], t40_lo, t40_hi);
        if (__all(ok) || ++spins >= SPIN_CAP) break;
        asm volatile("" ::: "memory");
      }
      float y0 = (val24(yg[0][0]) + val24(yg[1][0])) + (val24(yg[2][0]) + val24(yg[3][0]));
      float y1 = (val24(yg[0][2]) + val24(yg[1][2])) + (val24(yg[2][2]) + val24(yg[3][2]));
      y0 += dpp<ROR8>(y0); y0 = rbf(xrow_sum(y0));
      y1 += dpp<ROR8>(y1); y1 = rbf(xrow_sum(y1));
      if (lane < 8) *reinterpret_cast<f32x2*>(g_s + lane * 2) = f32x2{rbf(y0 / (1.f + __expf(-y0))), rbf(y1 / (1.f + __expf(-y1)))};
    }
  bf16x8 wof[2];
  {
    const unsigned short* ws16 = reinterpret_cast<const unsigned short*>(wo_s) + wave * 16 + n16;
#pragma unroll
    for (int s = 0; s < 2; ++s) {
      u32x4 t;
#pragma unroll
      for (int j = 0; j < 4; ++j)
        t[j] = (unsigned)ws16[(s * 32 + g4 * 8 + 2 * j) * (17 * 8)] | ((unsigned)ws16[(s * 32 + g4 * 8 + 2 * j + 1) * (17 * 8)] << 16);
      wof[s] = __builtin_bit_cast(bf16x8, t);
    }
  }
  {
    const unsigned o2 = WS_ST + (unsigned)((kvh * 4 + (idx >> 3)) * 128 + lane * 2) * 8;
    const unsigned o1 = WS_AP + (unsigned)((kvh + 8 * (lane >> 5) + 16 * ((lane >> 1) & 15)) * 512 + (idx >> 2) * 64 + wave * 8 + (lane & 1) * 4) * 8;
    u32x4 gs, ga, gb;
    unsigned spins = 0;
    if (wave == 0) {
      gs = LDG(o2);
      for (;;) {
        const bool okl = ok24x2(gs, t40_lo, t40_hi);
        if (__popcll(__ballot(!okl)) <= 2 * EARLY_GO || ++spins >= SPIN_CAP) break;
        __builtin_amdgcn_s_sleep(POLL_SLEEP);
        asm volatile("" ::: "memory");
        if (!okl) gs = LDG(o2);
      }
      SET_FLAG(8);
    } else {
      while (!GET_FLAG(8)) __builtin_amdgcn_s_sleep(0);
    }
    gs = LDG(o2); ga = LDG(o1); gb = LDG(o1 + 16);
    for (;;) {
      const bool okl = ok24x2(gs, t40_lo, t40_hi) & ok24x2(ga, t40_lo, t40_hi) & ok24x2(gb, t40_lo, t40_hi);
      if (__all(okl) || ++spins >= SPIN_CAP) break;
      asm volatile("" ::: "memory");
      if (!okl) { gs = LDG(o2); ga = LDG(o1); gb = LDG(o1 + 16); }
    }
    const float mine = __uint_as_float((gs[0] << 8) | ((lane & 1) ? 0u : (gs[2] & 0xffu)));
    const float other = dpp<XOR1>(mine);
    const float m = (lane & 1) ? other : mine, l = (lane & 1) ? mine : other;
    float M = m; M = fmaxf(M, dpp<XOR2>(M)); M = fmaxf(M, dpp<XOR7>(M)); M = fmaxf(M, dpp<ROR8>(M));
    { auto t = __builtin_amdgcn_permlane16_swap(__float_as_uint(M), __float_as_uint(M), false, false); M = fmaxf(__uint_as_float(t[0]), __uint_as_float(t[1])); }
    const float f = __builtin_amdgcn_exp2f((m - M) * SOFT_C);
    float L = f * l, a0 = f * val24(ga[0]), a1 = f * val24(ga[2]), a2 = f * val24(gb[0]), a3 = f * val24(gb[2]);
    a0 = tr16(a0, a2); a1 = tr16(a1, a3); L = tr16(L, L);
    a0 = tr8(a0, a1, (lane >> 3) & 1); L += dpp<ROR8>(L);
    a0 += dpp<XOR2>(a0); L += dpp<XOR2>(L);
    { const float up = dpp<ROR4>(a0), dn = dpp<ROR12>(a0); a0 += ((lane >> 2) & 1) ? up : dn; }
    L += dpp<XOR7>(L);
    const float o = a0 / L;
    if ((lane & 6) == 0) o_s[(lane >> 5) * 64 + wave * 8 + (lane & 1) * 4 + ((lane >> 4) & 1) * 2 + ((lane >> 3) & 1)] = (unsigned short)(cvtpk(o, o) & 0xffffu);
  }
  __syncthreads();
  STAMP(5);
  {
    f32x4 d = {0.f, 0.f, 0.f, 0.f};
#pragma unroll
    for (int s = 0; s < 2; ++s) {
      u32x4 av = *reinterpret_cast<const u32x4*>(o_s + (n16 & 1) * 64 + s * 32 + g4 * 8);
      if (n16 >= 2) av = u32x4{0u, 0u, 0u, 0u};
      d = __builtin_amdgcn_mfma_f32_16x16x32_bf16(__builtin_bit_cast(bf16x8, av), wof[s], d, 0, 0, 0);
    }
    if (lane < 16) {
      const unsigned so = WS_SP + (unsigned)((kvh * 8 + (idx >> 2)) * 1024 + ((idx & 3) * 8 + wave) * 32 + lane) * 8;
      __builtin_amdgcn_raw_buffer_store_b64(gr24(f24(d[0]), t40_lo, t40_hi), wsr, so, 0, AUX_SC1);
      __builtin_amdgcn_raw_buffer_store_b64(gr24(f24(d[1]), t40_lo, t40_hi), wsr, so + 128, 0, AUX_SC1);
    }
  }
  STAMP(6);
  if (w < 64) {
    const unsigned soff = WS_SP + (unsigned)((wave * 8 + frow) * 1024 + w * 16 + fcc * 2) * 8;
    const int oidx = (w & 1) * DM + (w >> 1) * 16 + fcc * 2;
    const unsigned xr = *reinterpret_cast<const unsigned*>(p.x + oidx);
    u32x4 sg;
    unsigned spins = 0;
    sg = LDG(soff);
    for (;;) {
      const bool okl = ok24x2(sg, t40_lo, t40_hi);
      if (__all(okl) || ++spins >= SPIN_CAP) break;
      asm volatile("" ::: "memory");
      if (!okl) sg = LDG(soff);
    }
    float s0 = val24(sg[0]), s1 = val24(sg[2]);
    s0 += dpp<ROR8>(s0); s0 = xrow_sum(s0);
    s1 += dpp<ROR8>(s1); s1 = xrow_sum(s1);
    if (lane < 8) *reinterpret_cast<f32x2*>(fin_s + wave * 16 + lane * 2) = f32x2{s0, s1};
    __syncthreads();
    if (wave == 0 && lane < 8) {
      float sa = 0.f, sb = 0.f;
#pragma unroll
      for (int v = 0; v < 8; ++v) { const f32x2 t = *reinterpret_cast<const f32x2*>(fin_s + v * 16 + lane * 2); sa += t[0]; sb += t[1]; }
      const f32x2 g = *reinterpret_cast<const f32x2*>(g_s + lane * 2);
      const float r0 = bf_lo(xr) + rbf(rbf(sa) * g[0]), r1 = bf_hi(xr) + rbf(rbf(sb) * g[1]);
      STAMP(7);
      if (w == 0 && lane == 0 && (float)(stv[7] - stv[0]) * 0.01f > THR) { const float tt = (float)(stv[REPORT] - stv[0]) * 0.01f; *reinterpret_cast<unsigned*>(p.out + oidx) = cvtpk(tt, r1); } else
      *reinterpret_cast<unsigned*>(p.out + oidx) = cvtpk(r0, r1);
      if (w == 0 && lane == 0) __hip_atomic_store(reinterpret_cast<u64*>(p.ws + WS_EPOCH), tag, __ATOMIC_RELAXED, __HIP_MEMORY_SCOPE_AGENT);
    }
  }
  STAMP(7);
}

}

extern "C" void kernel_launch(void* const* d_in, const int* in_sizes, int n_in, void* d_out, int out_size, void* d_ws, size_t ws_size,
                              hipStream_t stream) {
  Params p;
  p.x = (const unsigned short*)d_in[0];
  p.k = (const unsigned short*)d_in[1];
  p.v = (const unsigned short*)d_in[2];
  p.wq = (const unsigned short*)d_in[3];
  p.wo = (const unsigned short*)d_in[4];
  p.w1 = (const unsigned short*)d_in[6];
  p.w2 = (const unsigned short*)d_in[7];
  p.out = (unsigned short*)d_out;
  p.ws = (unsigned char*)d_ws;
  hipLaunchKernelGGL(fused_decode_kernel, dim3(NWG), dim3(NTHR), 0, stream, p);
}
```

```cpp
#include <hip/hip_runtime.h>
#include <hip/hip_bf16.h>
#include <stdint.h>

typedef unsigned u32x4 __attribute__((ext_vector_type(4)));
typedef unsigned u32x2 __attribute__((ext_vector_type(2)));
typedef float f32x4 __attribute__((ext_vector_type(4)));
typedef float f32x2 __attribute__((ext_vector_type(2)));
typedef __bf16 bf16x2 __attribute__((ext_vector_type(2)));
typedef __bf16 bf16x8 __attribute__((ext_vector_type(8)));
typedef short s16x4 __attribute__((ext_vector_type(4)));
typedef unsigned long long u64;

namespace {

constexpr int NWG = 256, NTHR = 512, NWAVE = 8;
constexpr int SK = 32768, DH = 128, DM = 512, DFF = 1792, NQ = 4096;
constexpr int DEPTH = 6;
constexpr int HT = DEPTH;
constexpr int NFULL = (32 - HT) / DEPTH, REM = (32 - HT) % DEPTH;
static_assert(4 * (NFULL * DEPTH + REM) + 4 * HT == 128, "128 tiles of 16 keys per workgroup");
constexpr int NFF = 224;
constexpr unsigned WS_EPOCH = 0;
constexpr unsigned WS_QG = 4096;
constexpr unsigned WS_HP = WS_QG + 4 * 2 * NQ * 8;
constexpr unsigned WS_AP = WS_HP + 8 * 28 * 128 * 8;
constexpr unsigned WS_ST = WS_AP + NWG * 4 * 128 * 8;
constexpr unsigned WS_YP = WS_ST + 8 * 4 * 32 * 4 * 8;
constexpr unsigned WS_SP = WS_YP + 28 * 1024 * 8;
constexpr unsigned WS_END = WS_SP + 64 * 1024 * 8;
constexpr unsigned SPIN_CAP = 1u << 16;
constexpr int Q_DELAY = 32, POLL_SLEEP = 16, EARLY_GO = 3, PRE_BAR = 2;
constexpr int AUX_NT = 2, AUX_SC1 = 16;
constexpr float SOFT_C = 0.08838834764831845f * 1.4426950408889634f;

__device__ __forceinline__ float bf_lo(unsigned u) { return __uint_as_float(u << 16); }
__device__ __forceinline__ float bf_hi(unsigned u) { return __uint_as_float(u & 0xffff0000u); }
__device__ __forceinline__ unsigned cvtpk(float lo, float hi) {
  return __builtin_bit_cast(unsigned, __builtin_convertvector(f32x2{lo, hi}, bf16x2));
}
__device__ __forceinline__ float rbf(float x) { return bf_lo(cvtpk(x, x)); }
template <int CTRL> __device__ __forceinline__ float dpp(float x) {
  return __builtin_bit_cast(float, __builtin_amdgcn_mov_dpp(__builtin_bit_cast(int, x), CTRL, 0xf, 0xf, true));
}
constexpr int ROR4 = 0x124, ROR8 = 0x128, ROR12 = 0x12C, XOR1 = 0xB1, XOR2 = 0x4E, XOR7 = 0x141;
__device__ __forceinline__ float xrow_max(float x) {
  auto s = __builtin_amdgcn_permlane16_swap(__float_as_uint(x), __float_as_uint(x), false, false);
  x = fmaxf(__uint_as_float(s[0]), __uint_as_float(s[1]));
  auto t = __builtin_amdgcn_permlane32_swap(__float_as_uint(x), __float_as_uint(x), false, false);
  return fmaxf(__uint_as_float(t[0]), __uint_as_float(t[1]));
}
__device__ __forceinline__ float xrow_sum(float x) {
  auto s = __builtin_amdgcn_permlane16_swap(__float_as_uint(x), __float_as_uint(x), false, false);
  x = __uint_as_float(s[0]) + __uint_as_float(s[1]);
  auto t = __builtin_amdgcn_permlane32_swap(__float_as_uint(x), __float_as_uint(x), false, false);
  return __uint_as_float(t[0]) + __uint_as_float(t[1]);
}
__device__ __forceinline__ float wave_sum(float x) {
  x += dpp<XOR1>(x); x += dpp<XOR2>(x); x += dpp<XOR7>(x); x += dpp<ROR8>(x);
  return xrow_sum(x);
}
__device__ __forceinline__ float tr32(float a, float b) {
  auto t = __builtin_amdgcn_permlane32_swap(__float_as_uint(a), __float_as_uint(b), false, false);
  return __uint_as_float(t[0]) + __uint_as_float(t[1]);
}
__device__ __forceinline__ float tr16(float a, float b) {
  auto t = __builtin_amdgcn_permlane16_swap(__float_as_uint(a), __float_as_uint(b), false, false);
  return __uint_as_float(t[0]) + __uint_as_float(t[1]);
}
__device__ __forceinline__ float tr8(float a, float b, bool bit3) {
  const float keep = bit3 ? b : a, send = bit3 ? a : b;
  return keep + dpp<ROR8>(send);
}
__device__ __forceinline__ unsigned f24(float x) { return (__float_as_uint(x) + 0x80u) >> 8; }
__device__ __forceinline__ u32x2 gr24(unsigned payload24, unsigned tlo8, unsigned thi) { return u32x2{payload24 | tlo8, thi}; }
__device__ __forceinline__ bool ok24(unsigned lo, unsigned hi, unsigned tlo8, unsigned thi) { return ((lo & 0xff000000u) == tlo8) & (hi == thi); }
__device__ __forceinline__ float val24(unsigned lo) { return __uint_as_float(lo << 8); }

struct Params {
  const unsigned short* x; const unsigned short* k; const unsigned short* v; const unsigned short* wq; const unsigned short* wo;
  const unsigned short* w1; const unsigned short* w2; unsigned short* out; unsigned char* ws;
};

#define LDNT(p_) __builtin_nontemporal_load(reinterpret_cast<const u32x4*>(p_))

__device__ __forceinline__ void layernorm_to_lds(const u32x4 xa, const u32x4 xb, float* dst, int lane) {
  float e0[8], e1[8];
#pragma unroll
  for (int j = 0; j < 4; ++j) { e0[2 * j] = bf_lo(xa[j]); e0[2 * j + 1] = bf_hi(xa[j]); e1[2 * j] = bf_lo(xb[j]); e1[2 * j + 1] = bf_hi(xb[j]); }
  float s0 = 0.f, s1 = 0.f;
#pragma unroll
  for (int j = 0; j < 8; ++j) { s0 += e0[j]; s1 += e1[j]; }
  s0 = wave_sum(s0) * (1.f / DM); s1 = wave_sum(s1) * (1.f / DM);
  float v0 = 0.f, v1 = 0.f;
#pragma unroll
  for (int j = 0; j < 8; ++j) { const float a = e0[j] - s0, b = e1[j] - s1; v0 = fmaf(a, a, v0); v1 = fmaf(b, b, v1); }
  v0 = wave_sum(v0) * (1.f / DM); v1 = wave_sum(v1) * (1.f / DM);
  const float m0 = rbf(s0), m1 = rbf(s1);
  const float d0 = 1.f / rbf(sqrtf(rbf(rbf(v0) + 1e-5f))), d1 = 1.f / rbf(sqrtf(rbf(rbf(v1) + 1e-5f)));
#pragma unroll
  for (int j = 0; j < 8; j += 2)
    *reinterpret_cast<f32x4*>(dst + (8 * lane + j) * 2) =
        f32x4{rbf(rbf(e0[j] - m0) * d0), rbf(rbf(e1[j] - m1) * d1), rbf(rbf(e0[j + 1] - m0) * d0), rbf(rbf(e1[j + 1] - m1) * d1)};
}

__device__ __forceinline__ bool ok24x2(const u32x4 g, unsigned tlo8, unsigned thi) { return ok24(g[0], g[1], tlo8, thi) & ok24(g[2], g[3], tlo8, thi); }
__device__ __forceinline__ u32x4 gr24x2(float a, float b, unsigned tlo8, unsigned thi) { return u32x4{f24(a) | tlo8, thi, f24(b) | tlo8, thi}; }

__global__ void __launch_bounds__(NTHR, 2) fused_decode_kernel(Params p) {
  __shared__ __attribute__((aligned(16))) float lds[13312];
  float* xn_s = lds;
  float* red_q = lds + 1024;
  float* mrg_m = lds + 1536;
  float* mrg_l = lds + 1568;
  unsigned short* o_s = reinterpret_cast<unsigned short*>(lds + 2560);
  float* fin_s = lds + 1632;
  float* tr_h = lds + 1760;
  float* h_s = lds + 1888;
  float* g_s = lds + 2016;
  unsigned* q_s = reinterpret_cast<unsigned*>(lds + 2048);
  unsigned* flag_w = reinterpret_cast<unsigned*>(lds + 2304);
  float* mrg_o = lds + 4608;
  u32x4* wo_s = reinterpret_cast<u32x4*>(lds + 8704);

  const int w = blockIdx.x, tid = threadIdx.x, lane = tid & 63;
  const int wave = __builtin_amdgcn_readfirstlane(tid >> 6);
  const int kvh = w & 7, bat = (w >> 3) & 1, split = w >> 4, idx = w >> 3;
  const int n16 = lane & 15, g4 = lane >> 4, frow = lane >> 3, fcc = lane & 7;

  const __amdgpu_buffer_rsrc_t wsr = __builtin_amdgcn_make_buffer_rsrc((void*)p.ws, 0, WS_END, 0x00020000);
#define LOAD_EPOCH() __hip_atomic_load(reinterpret_cast<const u64*>(p.ws + WS_EPOCH), __ATOMIC_RELAXED, __HIP_MEMORY_SCOPE_AGENT)
#define WG_BARRIER() do { __builtin_amdgcn_sched_barrier(0); asm volatile("s_waitcnt lgkmcnt(0)\n\ts_barrier" ::: "memory"); __builtin_amdgcn_sched_barrier(0); } while (0)
#define LDG(off_) __builtin_amdgcn_raw_buffer_load_b128(wsr, (off_), 0, AUX_SC1)
#define STG(v_, off_) __builtin_amdgcn_raw_buffer_store_b128((v_), wsr, (off_), 0, AUX_SC1)
  const bool streamer = wave >= 4;
  const int f2kb = (w & 7) + 8 * (w >> 6), f2cb = (w >> 3) & 7;

#define SET_FLAG(i_) __hip_atomic_store(&flag_w[i_], 1u, __ATOMIC_RELEASE, __HIP_MEMORY_SCOPE_WORKGROUP)
#define GET_FLAG(i_) (__hip_atomic_load(&flag_w[i_], __ATOMIC_ACQUIRE, __HIP_MEMORY_SCOPE_WORKGROUP) != 0u)
  const __amdgpu_buffer_rsrc_t kr = __builtin_amdgcn_make_buffer_rsrc((void*)p.k, 0, 2u * 8 * SK * DH * 2, 0x00020000);
  const __amdgpu_buffer_rsrc_t vr = __builtin_amdgcn_make_buffer_rsrc((void*)p.v, 0, 2u * 8 * SK * DH * 2, 0x00020000);
  const unsigned kvoff = (unsigned)(((n16 & 3) * 4 + (n16 >> 2)) * 256 + g4 * 16);
  const unsigned vvoff = (unsigned)lane * 16;
  const unsigned sbase = (unsigned)((bat * 8 + kvh) * SK + split * 2048 + ((streamer ? 4 * HT : 0) + (wave & 3)) * 16) * (DH * 2);
  constexpr unsigned TSTEP = 4 * 16 * DH * 2;
  u32x4 kb[DEPTH][4], vb[DEPTH][4];
#define ISSUE(d_, so_) do { \
    _Pragma("unroll") for (int s_ = 0; s_ < 4; ++s_) kb[d_][s_] = __builtin_amdgcn_raw_buffer_load_b128(kr, kvoff + s_ * 64, (so_), AUX_NT); \
    _Pragma("unroll") for (int s_ = 0; s_ < 4; ++s_) vb[d_][s_] = __builtin_amdgcn_raw_buffer_load_b128(vr, vvoff + s_ * 1024, (so_), AUX_NT); \
    __builtin_amdgcn_sched_barrier(0); } while (0)
  u64 tag;
  unsigned xr = 0u;
  if (__builtin_expect(streamer, 1)) {
#pragma unroll
    for (int d = 0; d < PRE_BAR; ++d) ISSUE(d, sbase + d * TSTEP);
    WG_BARRIER();
#pragma unroll
    for (int d = PRE_BAR; d < DEPTH; ++d) ISSUE(d, sbase + d * TSTEP);
    tag = LOAD_EPOCH() + 1;
  } else {
    const u32x4 xa = *reinterpret_cast<const u32x4*>(p.x + 8 * lane);
    const u32x4 xb = *reinterpret_cast<const u32x4*>(p.x + DM + 8 * lane);
    tag = LOAD_EPOCH() + 1;
    xr = *reinterpret_cast<const unsigned*>(p.x + (w & 1) * DM + ((w & 63) >> 1) * 16 + fcc * 2);
    __builtin_amdgcn_sched_barrier(0);
    const int qrow = (w >> 6) * 128 + wave * 32 + frow;
    const unsigned short* wqp = p.wq + (size_t)qrow * NQ + (w & 63) * 64 + fcc * 8;
    u32x4 wv[4];
#pragma unroll
    for (int k = 0; k < 4; ++k) wv[k] = LDNT(wqp + (size_t)k * 8 * NQ);
    __builtin_amdgcn_sched_barrier(0);
    const bool has_ff = w < NFF;
    const int f1row = (w / 28) * 64 + frow;
    u32x4 wt[16];
    if (wave == 1) {
      if (has_ff) {
#pragma unroll
        for (int k = 0; k < 8; ++k) wt[k] = LDNT(p.w1 + (size_t)(f1row + 8 * k) * DFF + (w % 28) * 64 + fcc * 8);
      }
    } else if (wave == 2) {
#pragma unroll
      for (int k = 0; k < 16; ++k) wt[k] = LDNT(p.wo + (size_t)(kvh * 512 + (idx >> 2) * 64 + k * 4 + g4) * DM + (idx & 3) * 128 + n16 * 8);
    } else if (wave == 0) {
      if (f2kb < 28) {
#pragma unroll
        for (int k = 0; k < 8; ++k) wt[k] = LDNT(p.w2 + (size_t)(f2kb * 64 + frow + 8 * k) * DM + f2cb * 64 + fcc * 8);
      }
    }
    if (tid < 16) flag_w[tid] = 0u;
    WG_BARRIER();
    layernorm_to_lds(xa, xb, xn_s, lane);
    const unsigned t40_lo = (unsigned)(tag << 24), t40_hi = (unsigned)(tag >> 8);
    const bool b3 = (lane >> 3) & 1;
    {
      float qa[16];
#pragma unroll
      for (int j = 0; j < 16; ++j) qa[j] = 0.f;
#pragma unroll
      for (int k = 0; k < 4; ++k) {
        const f32x2 xv = *reinterpret_cast<const f32x2*>(xn_s + (qrow + 8 * k) * 2);
#pragma unroll
        for (int j = 0; j < 4; ++j) {
          const float l0 = bf_lo(wv[k][j]), h0 = bf_hi(wv[k][j]);
          qa[2 * j] = fmaf(xv[0], l0, qa[2 * j]); qa[2 * j + 1] = fmaf(xv[0], h0, qa[2 * j + 1]);
          qa[8 + 2 * j] = fmaf(xv[1], l0, qa[8 + 2 * j]); qa[8 + 2 * j + 1] = fmaf(xv[1], h0, qa[8 + 2 * j + 1]);
        }
      }
#pragma unroll
      for (int j = 0; j < 8; ++j) qa[j] = tr32(qa[j], qa[j + 8]);
#pragma unroll
      for (int j = 0; j < 4; ++j) qa[j] = tr16(qa[j], qa[j + 4]);
      *reinterpret_cast<f32x2*>(red_q + wave * 128 + (lane >> 5) * 64 + fcc * 8 + ((lane >> 3) & 3) * 2) = f32x2{tr8(qa[0], qa[2], b3), tr8(qa[1], qa[3], b3)};
      SET_FLAG(wave);
    }
    if (wave == 0) {
      while (!__all(GET_FLAG(lane & 3))) __builtin_amdgcn_s_sleep(0);
      f32x2 t = *reinterpret_cast<const f32x2*>(red_q + 2 * lane);
#pragma unroll
      for (int v = 1; v < 4; ++v) t += *reinterpret_cast<const f32x2*>(red_q + v * 128 + 2 * lane);
      STG(gr24x2(t[0], t[1], t40_lo, t40_hi), WS_QG + (unsigned)(((w >> 6) * 2 + (lane >> 5)) * NQ + (w & 63) * 64 + (lane & 31) * 2) * 8);
    } else if (wave == 1) {
      if (has_ff) {
        float ha[16];
#pragma unroll
        for (int j = 0; j < 16; ++j) ha[j] = 0.f;
#pragma unroll
        for (int k = 0; k < 8; ++k) {
          const f32x2 xv = *reinterpret_cast<const f32x2*>(xn_s + (f1row + 8 * k) * 2);
#pragma unroll
          for (int j = 0; j < 4; ++j) {
            const float l0 = bf_lo(wt[k][j]), h0 = bf_hi(wt[k][j]);
            ha[2 * j] = fmaf(xv[0], l0, ha[2 * j]); ha[2 * j + 1] = fmaf(xv[0], h0, ha[2 * j + 1]);
            ha[8 + 2 * j] = fmaf(xv[1], l0, ha[8 + 2 * j]); ha[8 + 2 * j + 1] = fmaf(xv[1], h0, ha[8 + 2 * j + 1]);
          }
        }
#pragma unroll
        for (int j = 0; j < 8; ++j) ha[j] = tr32(ha[j], ha[j + 8]);
#pragma unroll
        for (int j = 0; j < 4; ++j) ha[j] = tr16(ha[j], ha[j + 4]);
        { const int u = fcc * 8 + (frow & 3) * 2;
          tr_h[u * 2 + (lane >> 5)] = tr8(ha[0], ha[2], b3); tr_h[u * 2 + 2 + (lane >> 5)] = tr8(ha[1], ha[3], b3); }
        const f32x2 t = *reinterpret_cast<const f32x2*>(tr_h + 2 * lane);
        STG(gr24x2(t[0], t[1], t40_lo, t40_hi), WS_HP + (unsigned)(w * 128 + 2 * lane) * 8);
      }
    } else if (wave == 2) {
#pragma unroll
      for (int k = 0; k < 16; ++k) wo_s[(k * 4 + g4) * 17 + n16] = wt[k];
    }
    __builtin_amdgcn_sched_barrier(0);
    if (wave == 0) {
      const unsigned qoff = WS_QG + (unsigned)(bat * NQ + kvh * 512 + 2 * lane) * 8;
      u32x4 qg[16];
      unsigned spins = 0;
      __builtin_amdgcn_s_sleep(Q_DELAY);
#define QLD(h_) _Pragma("unroll") for (int k = 0; k < 4; ++k) qg[(h_) * 4 + k] = LDG(qoff + (h_) * 1024 + k * (2 * NQ * 8))
#define QOK(h_) __all(ok24x2(qg[(h_) * 4], t40_lo, t40_hi) & ok24x2(qg[(h_) * 4 + 1], t40_lo, t40_hi) & ok24x2(qg[(h_) * 4 + 2], t40_lo, t40_hi) & ok24x2(qg[(h_) * 4 + 3], t40_lo, t40_hi))
      QLD(0); QLD(1); QLD(2); QLD(3);
      for (;;) {
        const bool k0 = QOK(0), k1 = QOK(1), k2 = QOK(2), k3 = QOK(3);
        if ((k0 & k1 & k2 & k3) || ++spins >= SPIN_CAP) break;
        __builtin_amdgcn_s_sleep(8);
        asm volatile("" ::: "memory");
        if (!k0) { QLD(0); }
        if (!k1) { QLD(1); }
        if (!k2) { QLD(2); }
        if (!k3) { QLD(3); }
      }
#undef QLD
#undef QOK
#pragma unroll
      for (int h = 0; h < 4; ++h)
        q_s[h * 64 + lane] = cvtpk((val24(qg[h * 4][0]) + val24(qg[h * 4 + 1][0])) + (val24(qg[h * 4 + 2][0]) + val24(qg[h * 4 + 3][0])),
                                   (val24(qg[h * 4][2]) + val24(qg[h * 4 + 1][2])) + (val24(qg[h * 4 + 2][2]) + val24(qg[h * 4 + 3][2])));
      SET_FLAG(4);
    }
    if (wave == 0) {
    if (f2kb < 28) {
      const unsigned hoff = WS_HP + (unsigned)(f2kb * 128 + 2 * lane) * 8;
      u32x4 hg[8];
      unsigned spins = 0;
#pragma unroll
      for (int k = 0; k < 8; ++k) hg[k] = LDG(hoff + k * (28 * 128 * 8));
      for (;;) {
        bool ok = true;
#pragma unroll
        for (int k = 0; k < 8; ++k) ok &= ok24x2(hg[k], t40_lo, t40_hi);
        if (__all(ok) || ++spins >= SPIN_CAP) break;
        __builtin_amdgcn_s_sleep(8);
        asm volatile("" ::: "memory");
#pragma unroll
        for (int k = 0; k < 8; ++k) hg[k] = LDG(hoff + k * (28 * 128 * 8));
      }
      float h0 = 0.f, h1 = 0.f;
#pragma unroll
      for (int k = 0; k < 8; ++k) { h0 += val24(hg[k][0]); h1 += val24(hg[k][2]); }
      *reinterpret_cast<f32x2*>(h_s + 2 * lane) = f32x2{fmaxf(rbf(h0), 0.f), fmaxf(rbf(h1), 0.f)};
      float ya[16];
#pragma unroll
      for (int j = 0; j < 16; ++j) ya[j] = 0.f;
#pragma unroll
      for (int k = 0; k < 8; ++k) {
        const f32x2 hv = *reinterpret_cast<const f32x2*>(h_s + (frow + 8 * k) * 2);
#pragma unroll
        for (int j = 0; j < 4; ++j) {
          const float l0 = bf_lo(wt[k][j]), hh = bf_hi(wt[k][j]);
          ya[2 * j] = fmaf(hv[0], l0, ya[2 * j]); ya[2 * j + 1] = fmaf(hv[0], hh, ya[2 * j + 1]);
          ya[8 + 2 * j] = fmaf(hv[1], l0, ya[8 + 2 * j]); ya[8 + 2 * j + 1] = fmaf(hv[1], hh, ya[8 + 2 * j + 1]);
        }
      }
#pragma unroll
      for (int j = 0; j < 8; ++j) ya[j] = tr32(ya[j], ya[j + 8]);
#pragma unroll
      for (int j = 0; j < 4; ++j) ya[j] = tr16(ya[j], ya[j + 4]);
      asm volatile("" ::: "memory");
      *reinterpret_cast<f32x2*>(h_s + ((fcc >> 1) * 2 + (lane >> 5)) * 16 + (fcc & 1) * 8 + (frow & 3) * 2) = f32x2{tr8(ya[0], ya[2], b3), tr8(ya[1], ya[3], b3)};
      const f32x2 t = *reinterpret_cast<const f32x2*>(h_s + 2 * lane);
      STG(gr24x2(t[0], t[1], t40_lo, t40_hi), WS_YP + (unsigned)(f2kb * 1024 + f2cb * 128 + 2 * lane) * 8);
    }
    }
    __builtin_amdgcn_sched_barrier(0);
#pragma unroll
    for (int d = 0; d < HT; ++d) ISSUE(d, sbase + d * TSTEP);
  }


  {
    const unsigned msk0 = (n16 & 3) == 0 ? 0x0000ffffu : (n16 & 3) == 1 ? 0xffff0000u : 0u;
    const unsigned msk1 = (n16 & 3) == 2 ? 0x0000ffffu : (n16 & 3) == 3 ? 0xffff0000u : 0u;
    float m_run = -INFINITY, l_run = 0.f;
    f32x4 olo = {0.f, 0.f, 0.f, 0.f}, ohi = {0.f, 0.f, 0.f, 0.f};
    while (!GET_FLAG(4)) __builtin_amdgcn_s_sleep(1);
    bf16x8 qf[4];
#pragma unroll
    for (int s = 0; s < 4; ++s) qf[s] = *reinterpret_cast<const bf16x8*>(q_s + ((n16 >> 2) * DH + s * 32 + g4 * 8) / 2);
#define TILE(d) do { \
      f32x4 sc = {0.f, 0.f, 0.f, 0.f}; \
      _Pragma("unroll") for (int s = 0; s < 4; ++s) sc = __builtin_amdgcn_mfma_f32_16x16x32_bf16(__builtin_bit_cast(bf16x8, kb[d][s]), qf[s], sc, 0, 0, 0); \
      float tmax = fmaxf(fmaxf(sc[0], sc[1]), fmaxf(sc[2], sc[3])); \
      tmax = xrow_max(tmax); \
      const float mnew = fmaxf(m_run, tmax); \
      const float alpha = __builtin_amdgcn_exp2f((m_run - mnew) * SOFT_C); \
      const float mc = -mnew * SOFT_C; \
      float pr[4]; \
      _Pragma("unroll") for (int r = 0; r < 4; ++r) pr[r] = __builtin_amdgcn_exp2f(fmaf(sc[r], SOFT_C, mc)); \
      l_run = fmaf(l_run, alpha, (pr[0] + pr[1]) + (pr[2] + pr[3])); \
      m_run = mnew; \
      olo *= alpha; ohi *= alpha; \
      _Pragma("unroll") for (int r = 0; r < 4; ++r) { \
        const unsigned pk = cvtpk(pr[r], pr[r]); \
        const u32x2 bp = {pk & msk0, pk & msk1}; \
        const u32x2 vlo = {vb[d][r][0], vb[d][r][1]}, vhi = {vb[d][r][2], vb[d][r][3]}; \
        olo = __builtin_amdgcn_mfma_f32_16x16x16bf16_1k(__builtin_bit_cast(s16x4, vlo), __builtin_bit_cast(s16x4, bp), olo, 0, 0, 0); \
        ohi = __builtin_amdgcn_mfma_f32_16x16x16bf16_1k(__builtin_bit_cast(s16x4, vhi), __builtin_bit_cast(s16x4, bp), ohi, 0, 0, 0); \
      } \
      __builtin_amdgcn_sched_barrier(0); } while (0)
    if (streamer) {
#pragma unroll
      for (int d = 0; d < REM; ++d) { TILE(d); ISSUE(d, sbase + (unsigned)(DEPTH + d) * TSTEP); }
#pragma unroll 1
      for (int it = 0; it < NFULL - 1; ++it) {
        const unsigned snext = sbase + (unsigned)(REM + (it + 1) * DEPTH) * TSTEP;
#pragma unroll
        for (int d = 0; d < DEPTH; ++d) {
          TILE((d + REM) % DEPTH);
          ISSUE((d + REM) % DEPTH, snext + d * TSTEP);
        }
      }
#pragma unroll
      for (int d = 0; d < DEPTH; ++d) TILE((d + REM) % DEPTH);
    } else {
#pragma unroll
      for (int d = 0; d < HT; ++d) TILE(d);
    }
#undef TILE
#undef ISSUE
    l_run = xrow_sum(l_run);
    float* mo = mrg_o + wave * 512 + (n16 >> 2) * DH + g4 * 32 + (n16 & 3);
#pragma unroll
    for (int r = 0; r < 4; ++r) { mo[r * 8] = olo[r]; mo[r * 8 + 4] = ohi[r]; }
    if (g4 == 0 && (n16 & 3) == 0) { mrg_m[wave * 4 + (n16 >> 2)] = m_run; mrg_l[wave * 4 + (n16 >> 2)] = l_run; }
  }
  const unsigned t40_lo = (unsigned)(tag << 24), t40_hi = (unsigned)(tag >> 8);
  asm volatile("" :: "v"(t40_lo), "v"(t40_hi), "v"(xr));
  __syncthreads();
  {
    const int h = tid >> 7;
    float mw[8], M = -INFINITY;
#pragma unroll
    for (int v = 0; v < 8; ++v) { mw[v] = mrg_m[v * 4 + h]; M = fmaxf(M, mw[v]); }
    float o = 0.f, L = 0.f;
#pragma unroll
    for (int v = 0; v < 8; ++v) {
      const float f = __builtin_amdgcn_exp2f((mw[v] - M) * SOFT_C);
      o = fmaf(f, mrg_o[v * 512 + tid], o); L = fmaf(f, mrg_l[v * 4 + h], L);
    }
    __builtin_amdgcn_raw_buffer_store_b64(gr24(f24(o), t40_lo, t40_hi), wsr, WS_AP + (unsigned)(w * 512 + tid) * 8, 0, AUX_SC1);
    if ((tid & 127) < 2) {
      const unsigned mb = __float_as_uint(M);
      const unsigned p0 = (tid & 1) ? f24(L) : (mb >> 8), p1 = (tid & 1) ? 0u : (mb & 0xffu);
      STG((u32x4{p0 | t40_lo, t40_hi, p1 | t40_lo, t40_hi}), WS_ST + (unsigned)(((kvh * 4 + h) * 32 + bat * 16 + split) * 4 + 2 * (tid & 1)) * 8);
    }
  }
  if (w < 64 && wave == 1) {
      const unsigned yoff = WS_YP + (unsigned)(frow * 1024 + w * 16 + fcc * 2) * 8;
      u32x4 yg[4];
      unsigned spins = 0;
      for (;;) {
        bool ok = true;
#pragma unroll
        for (int k = 0; k < 4; ++k) yg[k] = (k < 3 || lane < 32) ? LDG(yoff + k * 8 * 8192) : u32x4{t40_lo, t40_hi, t40_lo, t40_hi};
#pragma unroll
        for (int k = 0; k < 4; ++k) ok &= ok24x2(yg[k], t40_lo, t40_hi);
        if (__all(ok) || ++spins >= SPIN_CAP) break;
        asm volatile("" ::: "memory");
      }
      float y0 = (val24(yg[0][0]) + val24(yg[1][0])) + (val24(yg[2][0]) + val24(yg[3][0]));
      float y1 = (val24(yg[0][2]) + val24(yg[1][2])) + (val24(yg[2][2]) + val24(yg[3][2]));
      y0 += dpp<ROR8>(y0); y0 = rbf(xrow_sum(y0));
      y1 += dpp<ROR8>(y1); y1 = rbf(xrow_sum(y1));
      if (lane < 8) *reinterpret_cast<f32x2*>(g_s + lane * 2) = f32x2{rbf(y0 / (1.f + __expf(-y0))), rbf(y1 / (1.f + __expf(-y1)))};
    }
  bf16x8 wof[2];
  {
    const unsigned short* ws16 = reinterpret_cast<const unsigned short*>(wo_s) + wave * 16 + n16;
#pragma unroll
    for (int s = 0; s < 2; ++s) {
      u32x4 t;
#pragma unroll
      for (int j = 0; j < 4; ++j)
        t[j] = (unsigned)ws16[(s * 32 + g4 * 8 + 2 * j) * (17 * 8)] | ((unsigned)ws16[(s * 32 + g4 * 8 + 2 * j + 1) * (17 * 8)] << 16);
      wof[s] = __builtin_bit_cast(bf16x8, t);
    }
  }
  {
    const unsigned o2 = WS_ST + (unsigned)((kvh * 4 + (idx >> 3)) * 128 + lane * 2) * 8;
    const unsigned o1 = WS_AP + (unsigned)((kvh + 8 * (lane >> 5) + 16 * ((lane >> 1) & 15)) * 512 + (idx >> 2) * 64 + wave * 8 + (lane & 1) * 4) * 8;
    u32x4 gs, ga, gb;
    unsigned spins = 0;
    if (wave == 0) {
      gs = LDG(o2);
      for (;;) {
        const bool okl = ok24x2(gs, t40_lo, t40_hi);
        if (__popcll(__ballot(!okl)) <= 2 * EARLY_GO || ++spins >= SPIN_CAP) break;
        __builtin_amdgcn_s_sleep(POLL_SLEEP);
        asm volatile("" ::: "memory");
        if (!okl) gs = LDG(o2);
      }
      SET_FLAG(8);
    } else {
      while (!GET_FLAG(8)) __builtin_amdgcn_s_sleep(0);
    }
    gs = LDG(o2); ga = LDG(o1); gb = LDG(o1 + 16);
    for (;;) {
      const bool okl = ok24x2(gs, t40_lo, t40_hi) & ok24x2(ga, t40_lo, t40_hi) & ok24x2(gb, t40_lo, t40_hi);
      if (__all(okl) || ++spins >= SPIN_CAP) break;
      asm volatile("" ::: "memory");
      if (!okl) { gs = LDG(o2); ga = LDG(o1); gb = LDG(o1 + 16); }
    }
    const float mine = __uint_as_float((gs[0] << 8) | ((lane & 1) ? 0u : (gs[2] & 0xffu)));
    const float other = dpp<XOR1>(mine);
    const float m = (lane & 1) ? other : mine, l = (lane & 1) ? mine : other;
    float M = m; M = fmaxf(M, dpp<XOR2>(M)); M = fmaxf(M, dpp<XOR7>(M)); M = fmaxf(M, dpp<ROR8>(M));
    { auto t = __builtin_amdgcn_permlane16_swap(__float_as_uint(M), __float_as_uint(M), false, false); M = fmaxf(__uint_as_float(t[0]), __uint_as_float(t[1])); }
    const float f = __builtin_amdgcn_exp2f((m - M) * SOFT_C);
    float L = f * l, a0 = f * val24(ga[0]), a1 = f * val24(ga[2]), a2 = f * val24(gb[0]), a3 = f * val24(gb[2]);
    a0 = tr16(a0, a2); a1 = tr16(a1, a3); L = tr16(L, L);
    a0 = tr8(a0, a1, (lane >> 3) & 1); L += dpp<ROR8>(L);
    a0 += dpp<XOR2>(a0); L += dpp<XOR2>(L);
    { const float up = dpp<ROR4>(a0), dn = dpp<ROR12>(a0); a0 += ((lane >> 2) & 1) ? up : dn; }
    L += dpp<XOR7>(L);
    const float o = a0 / L;
    if ((lane & 6) == 0) o_s[(lane >> 5) * 64 + wave * 8 + (lane & 1) * 4 + ((lane >> 4) & 1) * 2 + ((lane >> 3) & 1)] = (unsigned short)(cvtpk(o, o) & 0xffffu);
  }
  __syncthreads();
  {
    f32x4 d = {0.f, 0.f, 0.f, 0.f};
#pragma unroll
    for (int s = 0; s < 2; ++s) {
      u32x4 av = *reinterpret_cast<const u32x4*>(o_s + (n16 & 1) * 64 + s * 32 + g4 * 8);
      if (n16 >= 2) av = u32x4{0u, 0u, 0u, 0u};
      d = __builtin_amdgcn_mfma_f32_16x16x32_bf16(__builtin_bit_cast(bf16x8, av), wof[s], d, 0, 0, 0);
    }
    if (lane < 16) {
      const unsigned so = WS_SP + (unsigned)((kvh * 8 + (idx >> 2)) * 1024 + ((idx & 3) * 8 + wave) * 32 + lane) * 8;
      __builtin_amdgcn_raw_buffer_store_b64(gr24(f24(d[0]), t40_lo, t40_hi), wsr, so, 0, AUX_SC1);
      __builtin_amdgcn_raw_buffer_store_b64(gr24(f24(d[1]), t40_lo, t40_hi), wsr, so + 128, 0, AUX_SC1);
    }
  }
  if (w < 64) {
    const unsigned soff = WS_SP + (unsigned)((wave * 8 + frow) * 1024 + w * 16 + fcc * 2) * 8;
    const int oidx = (w & 1) * DM + (w >> 1) * 16 + fcc * 2;
    u32x4 sg;
    unsigned spins = 0;
    sg = LDG(soff);
    for (;;) {
      const bool okl = ok24x2(sg, t40_lo, t40_hi);
      if (__all(okl) || ++spins >= SPIN_CAP) break;
      asm volatile("" ::: "memory");
      if (!okl) sg = LDG(soff);
    }
    float s0 = val24(sg[0]), s1 = val24(sg[2]);
    s0 += dpp<ROR8>(s0); s0 = xrow_sum(s0);
    s1 += dpp<ROR8>(s1); s1 = xrow_sum(s1);
    if (lane < 8) *reinterpret_cast<f32x2*>(fin_s + wave * 16 + lane * 2) = f32x2{s0, s1};
    __syncthreads();
    if (wave == 0 && lane < 8) {
      float sa = 0.f, sb = 0.f;
#pragma unroll
      for (int v = 0; v < 8; ++v) { const f32x2 t = *reinterpret_cast<const f32x2*>(fin_s + v * 16 + lane * 2); sa += t[0]; sb += t[1]; }
      const f32x2 g = *reinterpret_cast<const f32x2*>(g_s + lane * 2);
      const float r0 = bf_lo(xr) + rbf(rbf(sa) * g[0]), r1 = bf_hi(xr) + rbf(rbf(sb) * g[1]);
      *reinterpret_cast<unsigned*>(p.out + oidx) = cvtpk(r0, r1);
      if (w == 0 && lane == 0) __hip_atomic_store(reinterpret_cast<u64*>(p.ws + WS_EPOCH), tag, __ATOMIC_RELAXED, __HIP_MEMORY_SCOPE_AGENT);
    }
  }
}

}

extern "C" void kernel_launch(void* const* d_in, const int* in_sizes, int n_in, void* d_out, int out_size, void* d_ws, size_t ws_size,
                              hipStream_t stream) {
  Params p;
  p.x = (const unsigned short*)d_in[0];
  p.k = (const unsigned short*)d_in[1];
  p.v = (const unsigned short*)d_in[2];
  p.wq = (const unsigned short*)d_in[3];
  p.wo = (const unsigned short*)d_in[4];
  p.w1 = (const unsigned short*)d_in[6];
  p.w2 = (const unsigned short*)d_in[7];
  p.out = (unsigned short*)d_out;
  p.ws = (unsigned char*)d_ws;
  hipLaunchKernelGGL(fused_decode_kernel, dim3(NWG), dim3(NTHR), 0, stream, p);
}
```

```cpp
#include <hip/hip_runtime.h>
#include <hip/hip_bf16.h>
#include <stdint.h>

typedef unsigned u32x4 __attribute__((ext_vector_type(4)));
typedef unsigned u32x2 __attribute__((ext_vector_type(2)));
typedef float f32x4 __attribute__((ext_vector_type(4)));
typedef float f32x2 __attribute__((ext_vector_type(2)));
typedef __bf16 bf16x2 __attribute__((ext_vector_type(2)));
typedef __bf16 bf16x8 __attribute__((ext_vector_type(8)));
typedef short s16x4 __attribute__((ext_vector_type(4)));
typedef unsigned long long u64;

namespace {

constexpr int NWG = 256, NTHR = 512, NWAVE = 8;
constexpr int SK = 32768, DH = 128, DM = 512, DFF = 1792, NQ = 4096;
constexpr int DEPTH = 6;
constexpr int HT = DEPTH;
constexpr int NFULL = (32 - HT) / DEPTH, REM = (32 - HT) % DEPTH;
static_assert(4 * (NFULL * DEPTH + REM) + 4 * HT == 128, "128 tiles of 16 keys per workgroup");
constexpr int NFF = 224;
constexpr unsigned WS_EPOCH = 0;
constexpr unsigned WS_QG = 4096;
constexpr unsigned WS_HP = WS_QG + 4 * 2 * NQ * 8;
constexpr unsigned WS_AP = WS_HP + 8 * 28 * 128 * 8;
constexpr unsigned WS_ST = WS_AP + NWG * 4 * 128 * 8;
constexpr unsigned WS_YP = WS_ST + 8 * 4 * 32 * 4 * 8;
constexpr unsigned WS_SP = WS_YP + 28 * 1024 * 8;
constexpr unsigned WS_END = WS_SP + 64 * 1024 * 8;
constexpr unsigned SPIN_CAP = 1u << 16;
constexpr int Q_DELAY = 32, POLL_SLEEP = 16, EARLY_GO = 3, PRE_BAR = 2;
constexpr int AUX_NT = 2, AUX_SC1 = 16;
constexpr float SOFT_C = 0.08838834764831845f * 1.4426950408889634f;

__device__ __forceinline__ float bf_lo(unsigned u) { return __uint_as_float(u << 16); }
__device__ __forceinline__ float bf_hi(unsigned u) { return __uint_as_float(u & 0xffff0000u); }
__device__ __forceinline__ unsigned cvtpk(float lo, float hi) {
  return __builtin_bit_cast(unsigned, __builtin_convertvector(f32x2{lo, hi}, bf16x2));
}
__device__ __forceinline__ float rbf(float x) { return bf_lo(cvtpk(x, x)); }
template <int CTRL> __device__ __forceinline__ float dpp(float x) {
  return __builtin_bit_cast(float, __builtin_amdgcn_mov_dpp(__builtin_bit_cast(int, x), CTRL, 0xf, 0xf, true));
}
constexpr int ROR4 = 0x124, ROR8 = 0x128, ROR12 = 0x12C, XOR1 = 0xB1, XOR2 = 0x4E, XOR7 = 0x141;
__device__ __forceinline__ float xrow_max(float x) {
  auto s = __builtin_amdgcn_permlane16_swap(__float_as_uint(x), __float_as_uint(x), false, false);
  x = fmaxf(__uint_as_float(s[0]), __uint_as_float(s[1]));
  auto t = __builtin_amdgcn_permlane32_swap(__float_as_uint(x), __float_as_uint(x), false, false);
  return fmaxf(__uint_as_float(t[0]), __uint_as_float(t[1]));
}
__device__ __forceinline__ float xrow_sum(float x) {
  auto s = __builtin_amdgcn_permlane16_swap(__float_as_uint(x), __float_as_uint(x), false, false);
  x = __uint_as_float(s[0]) + __uint_as_float(s[1]);
  auto t = __builtin_amdgcn_permlane32_swap(__float_as_uint(x), __float_as_uint(x), false, false);
  return __uint_as_float(t[0]) + __uint_as_float(t[1]);
}
__device__ __forceinline__ float wave_sum(float x) {
  x += dpp<XOR1>(x); x += dpp<XOR2>(x); x += dpp<XOR7>(x); x += dpp<ROR8>(x);
  return xrow_sum(x);
}
__device__ __forceinline__ float tr32(float a, float b) {
  auto t = __builtin_amdgcn_permlane32_swap(__float_as_uint(a), __float_as_uint(b), false, false);
  return __uint_as_float(t[0]) + __uint_as_float(t[1]);
}
__device__ __forceinline__ float tr16(float a, float b) {
  auto t = __builtin_amdgcn_permlane16_swap(__float_as_uint(a), __float_as_uint(b), false, false);
  return __uint_as_float(t[0]) + __uint_as_float(t[1]);
}
__device__ __forceinline__ float tr8(float a, float b, bool bit3) {
  const float keep = bit3 ? b : a, send = bit3 ? a : b;
  return keep + dpp<ROR8>(send);
}
__device__ __forceinline__ unsigned f24(float x) { return (__float_as_uint(x) + 0x80u) >> 8; }
__device__ __forceinline__ u32x2 gr24(unsigned payload24, unsigned tlo8, unsigned thi) { return u32x2{payload24 | tlo8, thi}; }
__device__ __forceinline__ bool ok24(unsigned lo, unsigned hi, unsigned tlo8, unsigned thi) { return ((lo & 0xff000000u) == tlo8) & (hi == thi); }
__device__ __forceinline__ float val24(unsigned lo) { return __uint_as_float(lo << 8); }

struct Params {
  const unsigned short* x; const unsigned short* k; const unsigned short* v; const unsigned short* wq; const unsigned short* wo;
  const unsigned short* w1; const unsigned short* w2; unsigned short* out; unsigned char* ws;
};

#define LDNT(p_) __builtin_nontemporal_load(reinterpret_cast<const u32x4*>(p_))

__device__ __forceinline__ void layernorm_to_lds(const u32x4 xa, const u32x4 xb, float* dst, int lane) {
  float e0[8], e1[8];
#pragma unroll
  for (int j = 0; j < 4; ++j) { e0[2 * j] = bf_lo(xa[j]); e0[2 * j + 1] = bf_hi(xa[j]); e1[2 * j] = bf_lo(xb[j]); e1[2 * j + 1] = bf_hi(xb[j]); }
  float s0 = 0.f, s1 = 0.f;
#pragma unroll
  for (int j = 0; j < 8; ++j) { s0 += e0[j]; s1 += e1[j]; }
  s0 = wave_sum(s0) * (1.f / DM); s1 = wave_sum(s1) * (1.f / DM);
  float v0 = 0.f, v1 = 0.f;
#pragma unroll
  for (int j = 0; j < 8; ++j) { const float a = e0[j] - s0, b = e1[j] - s1; v0 = fmaf(a, a, v0); v1 = fmaf(b, b, v1); }
  v0 = wave_sum(v0) * (1.f / DM); v1 = wave_sum(v1) * (1.f / DM);
  const float m0 = rbf(s0), m1 = rbf(s1);
  const float d0 = 1.f / rbf(sqrtf(rbf(rbf(v0) + 1e-5f))), d1 = 1.f / rbf(sqrtf(rbf(rbf(v1) + 1e-5f)));
#pragma unroll
  for (int j = 0; j < 8; j += 2)
    *reinterpret_cast<f32x4*>(dst + (8 * lane + j) * 2) =
        f32x4{rbf(rbf(e0[j] - m0) * d0), rbf(rbf(e1[j] - m1) * d1), rbf(rbf(e0[j + 1] - m0) * d0), rbf(rbf(e1[j + 1] - m1) * d1)};
}

__device__ __forceinline__ bool ok24x2(const u32x4 g, unsigned tlo8, unsigned thi) { return ok24(g[0], g[1], tlo8, thi) & ok24(g[2], g[3], tlo8, thi); }
__device__ __forceinline__ u32x4 gr24x2(float a, float b, unsigned tlo8, unsigned thi) { return u32x4{f24(a) | tlo8, thi, f24(b) | tlo8, thi}; }

__global__ void __launch_bounds__(NTHR, 2) fused_decode_kernel(Params p) {
  __shared__ __attribute__((aligned(16))) float lds[13312];
  float* xn_s = lds;
  float* red_q = lds + 1024;
  float* mrg_m = lds + 1536;
  float* mrg_l = lds + 1568;
  unsigned short* o_s = reinterpret_cast<unsigned short*>(lds + 2560);
  float* fin_s = lds + 1632;
  float* tr_h = lds + 1760;
  float* h_s = lds + 1888;
  float* g_s = lds + 2016;
  unsigned* q_s = reinterpret_cast<unsigned*>(lds + 2048);
  unsigned* flag_w = reinterpret_cast<unsigned*>(lds + 2304);
  float* mrg_o = lds + 4608;
  u32x4* wo_s = reinterpret_cast<u32x4*>(lds + 8704);

  const int w = blockIdx.x, tid = threadIdx.x, lane = tid & 63;
  const int wave = __builtin_amdgcn_readfirstlane(tid >> 6);
  const int kvh = w & 7, bat = (w >> 3) & 1, split = w >> 4, idx = w >> 3;
  const int n16 = lane & 15, g4 = lane >> 4, frow = lane >> 3, fcc = lane & 7;

  const __amdgpu_buffer_rsrc_t wsr = __builtin_amdgcn_make_buffer_rsrc((void*)p.ws, 0, WS_END, 0x00020000);
#define LOAD_EPOCH() __hip_atomic_load(reinterpret_cast<const u64*>(p.ws + WS_EPOCH), __ATOMIC_RELAXED, __HIP_MEMORY_SCOPE_AGENT)
#define WG_BARRIER() do { __builtin_amdgcn_sched_barrier(0); asm volatile("s_waitcnt lgkmcnt(0)\n\ts_barrier" ::: "memory"); __builtin_amdgcn_sched_barrier(0); } while (0)
#define LDG(off_) __builtin_amdgcn_raw_buffer_load_b128(wsr, (off_), 0, AUX_SC1)
#define STG(v_, off_) __builtin_amdgcn_raw_buffer_store_b128((v_), wsr, (off_), 0, AUX_SC1)
  const bool streamer = wave >= 4;
  const int f2kb = (w & 7) + 8 * (w >> 6), f2cb = (w >> 3) & 7;

#define SET_FLAG(i_) __hip_atomic_store(&flag_w[i_], 1u, __ATOMIC_RELEASE, __HIP_MEMORY_SCOPE_WORKGROUP)
#define GET_FLAG(i_) (__hip_atomic_load(&flag_w[i_], __ATOMIC_ACQUIRE, __HIP_MEMORY_SCOPE_WORKGROUP) != 0u)
  const __amdgpu_buffer_rsrc_t kr = __builtin_amdgcn_make_buffer_rsrc((void*)p.k, 0, 2u * 8 * SK * DH * 2, 0x00020000);
  const __amdgpu_buffer_rsrc_t vr = __builtin_amdgcn_make_buffer_rsrc((void*)p.v, 0, 2u * 8 * SK * DH * 2, 0x00020000);
  const unsigned kvoff = (unsigned)(((n16 & 3) * 4 + (n16 >> 2)) * 256 + g4 * 16);
  const unsigned vvoff = (unsigned)lane * 16;
  const unsigned sbase = (unsigned)((bat * 8 + kvh) * SK + split * 2048 + ((streamer ? 4 * HT : 0) + (wave & 3)) * 16) * (DH * 2);
  constexpr unsigned TSTEP = 4 * 16 * DH * 2;
  u32x4 kb[DEPTH][4], vb[DEPTH][4];
#define ISSUE(d_, so_) do { \
    _Pragma("unroll") for (int s_ = 0; s_ < 4; ++s_) kb[d_][s_] = __builtin_amdgcn_raw_buffer_load_b128(kr, kvoff + s_ * 64, (so_), AUX_NT); \
    _Pragma("unroll") for (int s_ = 0; s_ < 4; ++s_) vb[d_][s_] = __builtin_amdgcn_raw_buffer_load_b128(vr, vvoff + s_ * 1024, (so_), AUX_NT); \
    __builtin_amdgcn_sched_barrier(0); } while (0)
  u64 tag;
  unsigned xr = 0u;
  if (__builtin_expect(streamer, 1)) {
#pragma unroll
    for (int d = 0; d < PRE_BAR; ++d) ISSUE(d, sbase + d * TSTEP);
    WG_BARRIER();
#pragma unroll
    for (int d = PRE_BAR; d < DEPTH; ++d) ISSUE(d, sbase + d * TSTEP);
    tag = LOAD_EPOCH() + 1;
  } else {
    const u32x4 xa = *reinterpret_cast<const u32x4*>(p.x + 8 * lane);
    const u32x4 xb = *reinterpret_cast<const u32x4*>(p.x + DM + 8 * lane);
    tag = LOAD_EPOCH() + 1;
    xr = *reinterpret_cast<const unsigned*>(p.x + (w & 1) * DM + ((w & 63) >> 1) * 16 + fcc * 2);
    __builtin_amdgcn_sched_barrier(0);
    const int qrow = (w >> 6) * 128 + wave * 32 + frow;
    const unsigned short* wqp = p.wq + (size_t)qrow * NQ + (w & 63) * 64 + fcc * 8;
    u32x4 wv[4];
#pragma unroll
    for (int k = 0; k < 4; ++k) wv[k] = LDNT(wqp + (size_t)k * 8 * NQ);
    __builtin_amdgcn_sched_barrier(0);
    const bool has_ff = w < NFF;
    const int f1row = (w / 28) * 64 + frow;
    u32x4 wt[16];
    if (wave == 1) {
      if (has_ff) {
#pragma unroll
        for (int k = 0; k < 8; ++k) wt[k] = LDNT(p.w1 + (size_t)(f1row + 8 * k) * DFF + (w % 28) * 64 + fcc * 8);
      }
    } else if (wave == 2) {
#pragma unroll
      for (int k = 0; k < 16; ++k) wt[k] = LDNT(p.wo + (size_t)(kvh * 512 + (idx >> 2) * 64 + k * 4 + g4) * DM + (idx & 3) * 128 + n16 * 8);
    } else if (wave == 0) {
      if (f2kb < 28) {
#pragma unroll
        for (int k = 0; k < 8; ++k) wt[k] = LDNT(p.w2 + (size_t)(f2kb * 64 + frow + 8 * k) * DM + f2cb * 64 + fcc * 8);
      }
    }
    if (tid < 16) flag_w[tid] = 0u;
    WG_BARRIER();
    layernorm_to_lds(xa, xb, xn_s, lane);
    const unsigned t40_lo = (unsigned)(tag << 24), t40_hi = (unsigned)(tag >> 8);
    const bool b3 = (lane >> 3) & 1;
    {
      float qa[16];
#pragma unroll
      for (int j = 0; j < 16; ++j) qa[j] = 0.f;
#pragma unroll
      for (int k = 0; k < 4; ++k) {
        const f32x2 xv = *reinterpret_cast<const f32x2*>(xn_s + (qrow + 8 * k) * 2);
#pragma unroll
        for (int j = 0; j < 4; ++j) {
          const float l0 = bf_lo(wv[k][j]), h0 = bf_hi(wv[k][j]);
          qa[2 * j] = fmaf(xv[0], l0, qa[2 * j]); qa[2 * j + 1] = fmaf(xv[0], h0, qa[2 * j + 1]);
          qa[8 + 2 * j] = fmaf(xv[1], l0, qa[8 + 2 * j]); qa[8 + 2 * j + 1] = fmaf(xv[1], h0, qa[8 + 2 * j + 1]);
        }
      }
#pragma unroll
      for (int j = 0; j < 8; ++j) qa[j] = tr32(qa[j], qa[j + 8]);
#pragma unroll
      for (int j = 0; j < 4; ++j) qa[j] = tr16(qa[j], qa[j + 4]);
      *reinterpret_cast<f32x2*>(red_q + wave * 128 + (lane >> 5) * 64 + fcc * 8 + ((lane >> 3) & 3) * 2) = f32x2{tr8(qa[0], qa[2], b3), tr8(qa[1], qa[3], b3)};
      SET_FLAG(wave);
    }
    if (wave == 0) {
      while (!__all(GET_FLAG(lane & 3))) __builtin_amdgcn_s_sleep(0);
      f32x2 t = *reinterpret_cast<const f32x2*>(red_q + 2 * lane);
#pragma unroll
      for (int v = 1; v < 4; ++v) t += *reinterpret_cast<const f32x2*>(red_q + v * 128 + 2 * lane);
      STG(gr24x2(t[0], t[1], t40_lo, t40_hi), WS_QG + (unsigned)(((w >> 6) * 2 + (lane >> 5)) * NQ + (w & 63) * 64 + (lane & 31) * 2) * 8);
    } else if (wave == 1) {
      if (has_ff) {
        float ha[16];
#pragma unroll
        for (int j = 0; j < 16; ++j) ha[j] = 0.f;
#pragma unroll
        for (int k = 0; k < 8; ++k) {
          const f32x2 xv = *reinterpret_cast<const f32x2*>(xn_s + (f1row + 8 * k) * 2);
#pragma unroll
          for (int j = 0; j < 4; ++j) {
            const float l0 = bf_lo(wt[k][j]), h0 = bf_hi(wt[k][j]);
            ha[2 * j] = fmaf(xv[0], l0, ha[2 * j]); ha[2 * j + 1] = fmaf(xv[0], h0, ha[2 * j + 1]);
            ha[8 + 2 * j] = fmaf(xv[1], l0, ha[8 + 2 * j]); ha[8 + 2 * j + 1] = fmaf(xv[1], h0, ha[8 + 2 * j + 1]);
          }
        }
#pragma unroll
        for (int j = 0; j < 8; ++j) ha[j] = tr32(ha[j], ha[j + 8]);
#pragma unroll
        for (int j = 0; j < 4; ++j) ha[j] = tr16(ha[j], ha[j + 4]);
        { const int u = fcc * 8 + (frow & 3) * 2;
          tr_h[u * 2 + (lane >> 5)] = tr8(ha[0], ha[2], b3); tr_h[u * 2 + 2 + (lane >> 5)] = tr8(ha[1], ha[3], b3); }
        const f32x2 t = *reinterpret_cast<const f32x2*>(tr_h + 2 * lane);
        STG(gr24x2(t[0], t[1], t40_lo, t40_hi), WS_HP + (unsigned)(w * 128 + 2 * lane) * 8);
      }
    } else if (wave == 2) {
#pragma unroll
      for (int k = 0; k < 16; ++k) wo_s[(k * 4 + g4) * 17 + n16] = wt[k];
    }
    __builtin_amdgcn_sched_barrier(0);
    if (wave == 0) {
      const unsigned qoff = WS_QG + (unsigned)(bat * NQ + kvh * 512 + 2 * lane) * 8;
      u32x4 qg[16];
      unsigned spins = 0;
      __builtin_amdgcn_s_sleep(Q_DELAY);
#define QLD(h_) _Pragma("unroll") for (int k = 0; k < 4; ++k) qg[(h_) * 4 + k] = LDG(qoff + (h_) * 1024 + k * (2 * NQ * 8))
#define QOK(h_) __all(ok24x2(qg[(h_) * 4], t40_lo, t40_hi) & ok24x2(qg[(h_) * 4 + 1], t40_lo, t40_hi) & ok24x2(qg[(h_) * 4 + 2], t40_lo, t40_hi) & ok24x2(qg[(h_) * 4 + 3], t40_lo, t40_hi))
      QLD(0); QLD(1); QLD(2); QLD(3);
      for (;;) {
        const bool k0 = QOK(0), k1 = QOK(1), k2 = QOK(2), k3 = QOK(3);
        if ((k0 & k1 & k2 & k3) || ++spins >= SPIN_CAP) break;
        __builtin_amdgcn_s_sleep(8);
        asm volatile("" ::: "memory");
        if (!k0) { QLD(0); }
        if (!k1) { QLD(1); }
        if (!k2) { QLD(2); }
        if (!k3) { QLD(3); }
      }
#undef QLD
#undef QOK
#pragma unroll
      for (int h = 0; h < 4; ++h)
        q_s[h * 64 + lane] = cvtpk((val24(qg[h * 4][0]) + val24(qg[h * 4 + 1][0])) + (val24(qg[h * 4 + 2][0]) + val24(qg[h * 4 + 3][0])),
                                   (val24(qg[h * 4][2]) + val24(qg[h * 4 + 1][2])) + (val24(qg[h * 4 + 2][2]) + val24(qg[h * 4 + 3][2])));
      SET_FLAG(4);
    }
    if (wave == 0 && f2kb < 28) {
      const unsigned hoff = WS_HP + (unsigned)(f2kb * 128 + 2 * lane) * 8;
      u32x4 hg[8];
      unsigned spins = 0;
#pragma unroll
      for (int k = 0; k < 8; ++k) hg[k] = LDG(hoff + k * (28 * 128 * 8));
      for (;;) {
        bool ok = true;
#pragma unroll
        for (int k = 0; k < 8; ++k) ok &= ok24x2(hg[k], t40_lo, t40_hi);
        if (__all(ok) || ++spins >= SPIN_CAP) break;
        __builtin_amdgcn_s_sleep(8);
        asm volatile("" ::: "memory");
#pragma unroll
        for (int k = 0; k < 8; ++k) hg[k] = LDG(hoff + k * (28 * 128 * 8));
      }
      float h0 = 0.f, h1 = 0.f;
#pragma unroll
      for (int k = 0; k < 8; ++k) { h0 += val24(hg[k][0]); h1 += val24(hg[k][2]); }
      *reinterpret_cast<f32x2*>(h_s + 2 * lane) = f32x2{fmaxf(rbf(h0), 0.f), fmaxf(rbf(h1), 0.f)};
      float ya[16];
#pragma unroll
      for (int j = 0; j < 16; ++j) ya[j] = 0.f;
#pragma unroll
      for (int k = 0; k < 8; ++k) {
        const f32x2 hv = *reinterpret_cast<const f32x2*>(h_s + (frow + 8 * k) * 2);
#pragma unroll
        for (int j = 0; j < 4; ++j) {
          const float l0 = bf_lo(wt[k][j]), hh = bf_hi(wt[k][j]);
          ya[2 * j] = fmaf(hv[0], l0, ya[2 * j]); ya[2 * j + 1] = fmaf(hv[0], hh, ya[2 * j + 1]);
          ya[8 + 2 * j] = fmaf(hv[1], l0, ya[8 + 2 * j]); ya[8 + 2 * j + 1] = fmaf(hv[1], hh, ya[8 + 2 * j + 1]);
        }
      }
#pragma unroll
      for (int j = 0; j < 8; ++j) ya[j] = tr32(ya[j], ya[j + 8]);
#pragma unroll
      for (int j = 0; j < 4; ++j) ya[j] = tr16(ya[j], ya[j + 4]);
      asm volatile("" ::: "memory");
      *reinterpret_cast<f32x2*>(h_s + ((fcc >> 1) * 2 + (lane >> 5)) * 16 + (fcc & 1) * 8 + (frow & 3) * 2) = f32x2{tr8(ya[0], ya[2], b3), tr8(ya[1], ya[3], b3)};
      const f32x2 t = *reinterpret_cast<const f32x2*>(h_s + 2 * lane);
      STG(gr24x2(t[0], t[1], t40_lo, t40_hi), WS_YP + (unsigned)(f2kb * 1024 + f2cb * 128 + 2 * lane) * 8);
    }
    __builtin_amdgcn_sched_barrier(0);
#pragma unroll
    for (int d = 0; d < HT; ++d) ISSUE(d, sbase + d * TSTEP);
  }

  {
    const unsigned msk0 = (n16 & 3) == 0 ? 0x0000ffffu : (n16 & 3) == 1 ? 0xffff0000u : 0u;
    const unsigned msk1 = (n16 & 3) == 2 ? 0x0000ffffu : (n16 & 3) == 3 ? 0xffff0000u : 0u;
    float m_run = -INFINITY, l_run = 0.f;
    f32x4 olo = {0.f, 0.f, 0.f, 0.f}, ohi = {0.f, 0.f, 0.f, 0.f};
    while (!GET_FLAG(4)) __builtin_amdgcn_s_sleep(1);
    bf16x8 qf[4];
#pragma unroll
    for (int s = 0; s < 4; ++s) qf[s] = *reinterpret_cast<const bf16x8*>(q_s + ((n16 >> 2) * DH + s * 32 + g4 * 8) / 2);
#define TILE(d) do { \
      f32x4 sc = {0.f, 0.f, 0.f, 0.f}; \
      _Pragma("unroll") for (int s = 0; s < 4; ++s) sc = __builtin_amdgcn_mfma_f32_16x16x32_bf16(__builtin_bit_cast(bf16x8, kb[d][s]), qf[s], sc, 0, 0, 0); \
      float tmax = fmaxf(fmaxf(sc[0], sc[1]), fmaxf(sc[2], sc[3])); \
      tmax = xrow_max(tmax); \
      const float mnew = fmaxf(m_run, tmax); \
      const float alpha = __builtin_amdgcn_exp2f((m_run - mnew) * SOFT_C); \
      const float mc = -mnew * SOFT_C; \
      float pr[4]; \
      _Pragma("unroll") for (int r = 0; r < 4; ++r) pr[r] = __builtin_amdgcn_exp2f(fmaf(sc[r], SOFT_C, mc)); \
      l_run = fmaf(l_run, alpha, (pr[0] + pr[1]) + (pr[2] + pr[3])); \
      m_run = mnew; \
      olo *= alpha; ohi *= alpha; \
      _Pragma("unroll") for (int r = 0; r < 4; ++r) { \
        const unsigned pk = cvtpk(pr[r], pr[r]); \
        const u32x2 bp = {pk & msk0, pk & msk1}; \
        const u32x2 vlo = {vb[d][r][0], vb[d][r][1]}, vhi = {vb[d][r][2], vb[d][r][3]}; \
        olo = __builtin_amdgcn_mfma_f32_16x16x16bf16_1k(__builtin_bit_cast(s16x4, vlo), __builtin_bit_cast(s16x4, bp), olo, 0, 0, 0); \
        ohi = __builtin_amdgcn_mfma_f32_16x16x16bf16_1k(__builtin_bit_cast(s16x4, vhi), __builtin_bit_cast(s16x4, bp), ohi, 0, 0, 0); \
      } \
      __builtin_amdgcn_sched_barrier(0); } while (0)
    if (streamer) {
#pragma unroll
      for (int d = 0; d < REM; ++d) { TILE(d); ISSUE(d, sbase + (unsigned)(DEPTH + d) * TSTEP); }
#pragma unroll 1
      for (int it = 0; it < NFULL - 1; ++it) {
        const unsigned snext = sbase + (unsigned)(REM + (it + 1) * DEPTH) * TSTEP;
#pragma unroll
        for (int d = 0; d < DEPTH; ++d) {
          TILE((d + REM) % DEPTH);
          ISSUE((d + REM) % DEPTH, snext + d * TSTEP);
        }
      }
#pragma unroll
      for (int d = 0; d < DEPTH; ++d) TILE((d + REM) % DEPTH);
    } else {
#pragma unroll
      for (int d = 0; d < HT; ++d) TILE(d);
    }
#undef TILE
#undef ISSUE
    l_run = xrow_sum(l_run);
    float* mo = mrg_o + wave * 512 + (n16 >> 2) * DH + g4 * 32 + (n16 & 3);
#pragma unroll
    for (int r = 0; r < 4; ++r) { mo[r * 8] = olo[r]; mo[r * 8 + 4] = ohi[r]; }
    if (g4 == 0 && (n16 & 3) == 0) { mrg_m[wave * 4 + (n16 >> 2)] = m_run; mrg_l[wave * 4 + (n16 >> 2)] = l_run; }
  }
  const unsigned t40_lo = (unsigned)(tag << 24), t40_hi = (unsigned)(tag >> 8);
  asm volatile("" :: "v"(t40_lo), "v"(t40_hi), "v"(xr));
  __syncthreads();
  {
    const int h = tid >> 7;
    float mw[8], M = -INFINITY;
#pragma unroll
    for (int v = 0; v < 8; ++v) { mw[v] = mrg_m[v * 4 + h]; M = fmaxf(M, mw[v]); }
    float o = 0.f, L = 0.f;
#pragma unroll
    for (int v = 0; v < 8; ++v) {
      const float f = __builtin_amdgcn_exp2f((mw[v] - M) * SOFT_C);
      o = fmaf(f, mrg_o[v * 512 + tid], o); L = fmaf(f, mrg_l[v * 4 + h], L);
    }
    __builtin_amdgcn_raw_buffer_store_b64(gr24(f24(o), t40_lo, t40_hi), wsr, WS_AP + (unsigned)(w * 512 + tid) * 8, 0, AUX_SC1);
    if ((tid & 127) < 2) {
      const unsigned mb = __float_as_uint(M);
      const unsigned p0 = (tid & 1) ? f24(L) : (mb >> 8), p1 = (tid & 1) ? 0u : (mb & 0xffu);
      STG((u32x4{p0 | t40_lo, t40_hi, p1 | t40_lo, t40_hi}), WS_ST + (unsigned)(((kvh * 4 + h) * 32 + bat * 16 + split) * 4 + 2 * (tid & 1)) * 8);
    }
  }
  if (w < 64 && wave == 1) {
    const unsigned yoff = WS_YP + (unsigned)(frow * 1024 + w * 16 + fcc * 2) * 8;
    u32x4 yg[4];
    unsigned spins = 0;
    for (;;) {
      bool ok = true;
#pragma unroll
      for (int k = 0; k < 4; ++k) yg[k] = (k < 3 || lane < 32) ? LDG(yoff + k * 8 * 8192) : u32x4{t40_lo, t40_hi, t40_lo, t40_hi};
#pragma unroll
      for (int k = 0; k < 4; ++k) ok &= ok24x2(yg[k], t40_lo, t40_hi);
      if (__all(ok) || ++spins >= SPIN_CAP) break;
      asm volatile("" ::: "memory");
    }
    float y0 = (val24(yg[0][0]) + val24(yg[1][0])) + (val24(yg[2][0]) + val24(yg[3][0]));
    float y1 = (val24(yg[0][2]) + val24(yg[1][2])) + (val24(yg[2][2]) + val24(yg[3][2]));
    y0 += dpp<ROR8>(y0); y0 = rbf(xrow_sum(y0));
    y1 += dpp<ROR8>(y1); y1 = rbf(xrow_sum(y1));
    if (lane < 8) *reinterpret_cast<f32x2*>(g_s + lane * 2) = f32x2{rbf(y0 / (1.f + __expf(-y0))), rbf(y1 / (1.f + __expf(-y1)))};
  }
  bf16x8 wof[2];
  {
    const unsigned short* ws16 = reinterpret_cast<const unsigned short*>(wo_s) + wave * 16 + n16;
#pragma unroll
    for (int s = 0; s < 2; ++s) {
      u32x4 t;
#pragma unroll
      for (int j = 0; j < 4; ++j)
        t[j] = (unsigned)ws16[(s * 32 + g4 * 8 + 2 * j) * (17 * 8)] | ((unsigned)ws16[(s * 32 + g4 * 8 + 2 * j + 1) * (17 * 8)] << 16);
      wof[s] = __builtin_bit_cast(bf16x8, t);
    }
  }
  {
    const unsigned o2 = WS_ST + (unsigned)((kvh * 4 + (idx >> 3)) * 128 + lane * 2) * 8;
    const unsigned o1 = WS_AP + (unsigned)((kvh + 8 * (lane >> 5) + 16 * ((lane >> 1) & 15)) * 512 + (idx >> 2) * 64 + wave * 8 + (lane & 1) * 4) * 8;
    u32x4 gs, ga, gb;
    unsigned spins = 0;
    if (wave == 0) {
      gs = LDG(o2);
      for (;;) {
        const bool okl = ok24x2(gs, t40_lo, t40_hi);
        if (__popcll(__ballot(!okl)) <= 2 * EARLY_GO || ++spins >= SPIN_CAP) break;
        __builtin_amdgcn_s_sleep(POLL_SLEEP);
        asm volatile("" ::: "memory");
        if (!okl) gs = LDG(o2);
      }
      SET_FLAG(8);
    } else {
      while (!GET_FLAG(8)) __builtin_amdgcn_s_sleep(0);
    }
    gs = LDG(o2); ga = LDG(o1); gb = LDG(o1 + 16);
    for (;;) {
      const bool okl = ok24x2(gs, t40_lo, t40_hi) & ok24x2(ga, t40_lo, t40_hi) & ok24x2(gb, t40_lo, t40_hi);
      if (__all(okl) || ++spins >= SPIN_CAP) break;
      asm volatile("" ::: "memory");
      if (!okl) { gs = LDG(o2); ga = LDG(o1); gb = LDG(o1 + 16); }
    }
    const float mine = __uint_as_float((gs[0] << 8) | ((lane & 1) ? 0u : (gs[2] & 0xffu)));
    const float other = dpp<XOR1>(mine);
    const float m = (lane & 1) ? other : mine, l = (lane & 1) ? mine : other;
    float M = m; M = fmaxf(M, dpp<XOR2>(M)); M = fmaxf(M, dpp<XOR7>(M)); M = fmaxf(M, dpp<ROR8>(M));
    { auto t = __builtin_amdgcn_permlane16_swap(__float_as_uint(M), __float_as_uint(M), false, false); M = fmaxf(__uint_as_float(t[0]), __uint_as_float(t[1])); }
    const float f = __builtin_amdgcn_exp2f((m - M) * SOFT_C);
    float L = f * l, a0 = f * val24(ga[0]), a1 = f * val24(ga[2]), a2 = f * val24(gb[0]), a3 = f * val24(gb[2]);
    a0 = tr16(a0, a2); a1 = tr16(a1, a3); L = tr16(L, L);
    a0 = tr8(a0, a1, (lane >> 3) & 1); L += dpp<ROR8>(L);
    a0 += dpp<XOR2>(a0); L += dpp<XOR2>(L);
    { const float up = dpp<ROR4>(a0), dn = dpp<ROR12>(a0); a0 += ((lane >> 2) & 1) ? up : dn; }
    L += dpp<XOR7>(L);
    const float o = a0 / L;
    if ((lane & 6) == 0) o_s[(lane >> 5) * 64 + wave * 8 + (lane & 1) * 4 + ((lane >> 4) & 1) * 2 + ((lane >> 3) & 1)] = (unsigned short)(cvtpk(o, o) & 0xffffu);
  }
  __syncthreads();
  {
    f32x4 d = {0.f, 0.f, 0.f, 0.f};
#pragma unroll
    for (int s = 0; s < 2; ++s) {
      u32x4 av = *reinterpret_cast<const u32x4*>(o_s + (n16 & 1) * 64 + s * 32 + g4 * 8);
      if (n16 >= 2) av = u32x4{0u, 0u, 0u, 0u};
      d = __builtin_amdgcn_mfma_f32_16x16x32_bf16(__builtin_bit_cast(bf16x8, av), wof[s], d, 0, 0, 0);
    }
    if (lane < 16) {
      const unsigned so = WS_SP + (unsigned)((kvh * 8 + (idx >> 2)) * 1024 + ((idx & 3) * 8 + wave) * 32 + lane) * 8;
      __builtin_amdgcn_raw_buffer_store_b64(gr24(f24(d[0]), t40_lo, t40_hi), wsr, so, 0, AUX_SC1);
      __builtin_amdgcn_raw_buffer_store_b64(gr24(f24(d[1]), t40_lo, t40_hi), wsr, so + 128, 0, AUX_SC1);
    }
  }
  if (w < 64) {
    const unsigned soff = WS_SP + (unsigned)((wave * 8 + frow) * 1024 + w * 16 + fcc * 2) * 8;
    const int oidx = (w & 1) * DM + (w >> 1) * 16 + fcc * 2;
    u32x4 sg;
    unsigned spins = 0;
    sg = LDG(soff);
    for (;;) {
      const bool okl = ok24x2(sg, t40_lo, t40_hi);
      if (__all(okl) || ++spins >= SPIN_CAP) break;
      asm volatile("" ::: "memory");
      if (!okl) sg = LDG(soff);
    }
    float s0 = val24(sg[0]), s1 = val24(sg[2]);
    s0 += dpp<ROR8>(s0); s0 = xrow_sum(s0);
    s1 += dpp<ROR8>(s1); s1 = xrow_sum(s1);
    if (lane < 8) *reinterpret_cast<f32x2*>(fin_s + wave * 16 + lane * 2) = f32x2{s0, s1};
    __syncthreads();
    if (wave == 0 && lane < 8) {
      float sa = 0.f, sb = 0.f;
#pragma unroll
      for (int v = 0; v < 8; ++v) { const f32x2 t = *reinterpret_cast<const f32x2*>(fin_s + v * 16 + lane * 2); sa += t[0]; sb += t[1]; }
      const f32x2 g = *reinterpret_cast<const f32x2*>(g_s + lane * 2);
      const float r0 = bf_lo(xr) + rbf(rbf(sa) * g[0]), r1 = bf_hi(xr) + rbf(rbf(sb) * g[1]);
      *reinterpret_cast<unsigned*>(p.out + oidx) = cvtpk(r0, r1);
      if (w == 0 && lane == 0) __hip_atomic_store(reinterpret_cast<u64*>(p.ws + WS_EPOCH), tag, __ATOMIC_RELAXED, __HIP_MEMORY_SCOPE_AGENT);
    }
  }
}

}

extern "C" void kernel_launch(void* const* d_in, const int* in_sizes, int n_in, void* d_out, int out_size, void* d_ws, size_t ws_size,
                              hipStream_t stream) {
  Params p;
  p.x = (const unsigned short*)d_in[0];
  p.k = (const unsigned short*)d_in[1];
  p.v = (const unsigned short*)d_in[2];
  p.wq = (const unsigned short*)d_in[3];
  p.wo = (const unsigned short*)d_in[4];
  p.w1 = (const unsigned short*)d_in[6];
  p.w2 = (const unsigned short*)d_in[7];
  p.out = (unsigned short*)d_out;
  p.ws = (unsigned char*)d_ws;
  hipLaunchKernelGGL(fused_decode_kernel, dim3(NWG), dim3(NTHR), 0, stream, p);
}
```
